```python
import torch

def _softmax(x, axis=-1):
    return torch.softmax(x, dim=axis)
def _gelu(x):
    return torch.nn.functional.gelu(x, approximate="tanh")
def _silu(x):
    return torch.nn.functional.silu(x)
def _sigmoid(x):
    return torch.sigmoid(x)
def _layernorm(x, axis=-1, eps=1e-5):
    m = x.mean(dim=axis, keepdim=True)
    v = x.var(dim=axis, keepdim=True, unbiased=False)
    return (x - m) / torch.sqrt(v + eps)
def _scatter_add(tgt, idx, src, axis=0):
    return tgt.clone().index_add_(axis, idx, src)
def _segment_sum(src, seg, n):
    out = torch.zeros((n,) + src.shape[1:], dtype=src.dtype)
    return out.index_add_(0, seg, src)
def _pad(x, pw):
    flat = tuple(v for pair in reversed(pw) for v in pair)
    return torch.nn.functional.pad(x, flat)

kernel_name = 'recurrent_scan__mha_block__glu_gated_glu__prompt'

def setup_inputs():

    _w = (lambda a: (lambda t: t.to(torch.bfloat16) if t.is_floating_point() else t)(torch.as_tensor(a)))
    def _A_setup():
        B, S, D, H = 1, 2048, 512, 32
        return {
            "x": _w((lambda *s: torch.randn(*s, dtype=torch.bfloat16) * 0.05)(B, S, D)),
            "wqkv": _w((lambda *s: torch.randn(*s, dtype=torch.bfloat16) * 0.05)(D, 3 * D)),
            "wo": _w((lambda *s: torch.randn(*s, dtype=torch.bfloat16) * 0.05)(D, D)),
        }
    _A_in = _A_setup()

    def _B_setup():
        B, S, D, F = 1, 2048, 512, 1792
        return {
            "x": _w((lambda *s: torch.randn(*s, dtype=torch.bfloat16) * 0.05)(B, S, D)),
            "w_gate": _w((lambda *s: torch.randn(*s, dtype=torch.bfloat16) * 0.05)(D, F)),
            "w_up": _w((lambda *s: torch.randn(*s, dtype=torch.bfloat16) * 0.05)(D, F)),
            "w_down": _w((lambda *s: torch.randn(*s, dtype=torch.bfloat16) * 0.05)(F, D)),
        }
    _B_in = _B_setup()

    return {
        "A_x": _A_in["x"],
        "A_wqkv": _A_in["wqkv"],
        "A_wo": _A_in["wo"],
        "B_x": _B_in["x"],
        "B_w_gate": _B_in["w_gate"],
        "B_w_up": _B_in["w_up"],
        "B_w_down": _B_in["w_down"],
    }

def reference(A_x, A_wqkv, A_wo, B_x, B_w_gate, B_w_up, B_w_down):
    def _A_ref(x, wqkv, wo):
        B, S, D = x.shape
        H = 32
        Dh = D // H
        qkv = (lambda x, *d: x.permute(*d))((x @ wqkv).reshape(B, S, 3, H, Dh), 2, 0, 3, 1, 4)
        q, k, v = qkv[0], qkv[1], qkv[2]
        scores = torch.einsum("bhqd,bhkd->bhqk", q, k) * (Dh ** -0.5)
        p = _softmax(scores, axis=-1)
        o = torch.einsum("bhqk,bhkd->bhqd", p, v)
        return (lambda x, *d: x.permute(*d))(o, 0, 2, 1, 3).reshape(B, S, D) @ wo

    def _B_ref(x, w_gate, w_up, w_down):
        g = torch.einsum("bsd,df->bsf", x, w_gate)
        u = torch.einsum("bsd,df->bsf", x, w_up)
        h = _sigmoid(g) * u
        return torch.einsum("bsf,fd->bsd", h, w_down)

    x = A_x
    xn = _layernorm(x, axis=-1)
    s = _A_ref(xn, A_wqkv, A_wo)
    g = _silu(_B_ref(xn, B_w_gate, B_w_up, B_w_down))
    return x + s * g
```

```cpp
#include <hip/hip_runtime.h>
#include <hip/hip_bf16.h>
#include <stdint.h>

typedef short bf16x8 __attribute__((ext_vector_type(8)));
typedef short s16x4 __attribute__((ext_vector_type(4)));
typedef float f32x16 __attribute__((ext_vector_type(16)));
typedef float f32x2 __attribute__((ext_vector_type(2)));
typedef __bf16 bfx2 __attribute__((ext_vector_type(2)));
typedef unsigned u32x4 __attribute__((ext_vector_type(4)));
typedef unsigned u32x2 __attribute__((ext_vector_type(2)));
#define LAS __attribute__((address_space(3)))
#define GAS __attribute__((address_space(1)))
typedef LAS char* lptr;
typedef GAS const char* gcptr;
typedef GAS char* gptr;
typedef GAS unsigned gu32;

#define MFMA32(a, b, c) __builtin_amdgcn_mfma_f32_32x32x16_bf16((a), (b), (c), 0, 0, 0)
#define RLX_AGENT __ATOMIC_RELAXED, __HIP_MEMORY_SCOPE_AGENT
#define RLX_WG __ATOMIC_RELAXED, __HIP_MEMORY_SCOPE_WORKGROUP

constexpr size_t WS_XN = 0;
constexpr size_t WS_Q = 2u << 20;
constexpr size_t WS_K = 4u << 20;
constexpr size_t WS_V = 6u << 20;
constexpr size_t WS_O = 8u << 20;
constexpr size_t WS_DP = 10u << 20;
constexpr size_t WS_H = 14u << 20;
constexpr size_t WS_CNT = 22u << 20;
constexpr size_t H_BYTES = (size_t)64 * 112 * 1024;

__device__ __forceinline__ unsigned pk2(float lo, float hi) {
    f32x2 v = {lo, hi};
    return __builtin_bit_cast(unsigned, __builtin_convertvector(v, bfx2));
}
__device__ __forceinline__ float bf_lo(unsigned u) { return __uint_as_float(u << 16); }
__device__ __forceinline__ float bf_hi(unsigned u) { return __uint_as_float(u & 0xffff0000u); }
__device__ __forceinline__ float rbf(float f) { return bf_lo(pk2(f, f) & 0xffffu); }
__device__ __forceinline__ void glds16(gcptr sbase, unsigned voff, unsigned lds_base) {
    unsigned sv;
    asm volatile("s_mov_b32 %0, m0\n\ts_mov_b32 m0, %3\n\ts_nop 0\n\tglobal_load_lds_dwordx4 %1, %2\n\ts_mov_b32 m0, %0"
                 : "=&s"(sv) : "v"(voff), "s"(sbase), "s"(lds_base) : "memory");
}
__device__ __forceinline__ s16x4 trd(lptr p) {
    return __builtin_amdgcn_ds_read_tr16_b64_v4i16((LAS s16x4*)p);
}
__device__ __forceinline__ bf16x8 cat4(s16x4 a, s16x4 b) {
    return __builtin_shufflevector(a, b, 0, 1, 2, 3, 4, 5, 6, 7);
}
__device__ __forceinline__ u32x4 pack8(const f32x16& a, int o, float s) {
    u32x4 r;
    r[0] = pk2(a[o + 0] * s, a[o + 1] * s); r[1] = pk2(a[o + 2] * s, a[o + 3] * s);
    r[2] = pk2(a[o + 4] * s, a[o + 5] * s); r[3] = pk2(a[o + 6] * s, a[o + 7] * s);
    return r;
}
__device__ __forceinline__ u32x4 pack8n(const f32x16& a, int o) {
    u32x4 r;
    r[0] = pk2(a[o + 0], a[o + 1]); r[1] = pk2(a[o + 2], a[o + 3]);
    r[2] = pk2(a[o + 4], a[o + 5]); r[3] = pk2(a[o + 6], a[o + 7]);
    return r;
}
__device__ __forceinline__ float half_swap_add(float v) {
    auto r = __builtin_amdgcn_permlane32_swap(__float_as_uint(v), __float_as_uint(v), false, false);
    return __uint_as_float(r[0]) + __uint_as_float(r[1]);
}
__device__ __forceinline__ float half_swap_max(float v) {
    auto r = __builtin_amdgcn_permlane32_swap(__float_as_uint(v), __float_as_uint(v), false, false);
    return __builtin_fmaxf(__uint_as_float(r[0]), __uint_as_float(r[1]));
}
__device__ __forceinline__ float max16(const f32x16& s) {
    float a = __builtin_fmaxf(__builtin_fmaxf(s[0], s[1]), s[2]), b = __builtin_fmaxf(__builtin_fmaxf(s[3], s[4]), s[5]);
    a = __builtin_fmaxf(__builtin_fmaxf(a, s[6]), s[7]);   b = __builtin_fmaxf(__builtin_fmaxf(b, s[8]), s[9]);
    a = __builtin_fmaxf(__builtin_fmaxf(a, s[10]), s[11]); b = __builtin_fmaxf(__builtin_fmaxf(b, s[12]), s[13]);
    a = __builtin_fmaxf(__builtin_fmaxf(a, s[14]), s[15]);
    return __builtin_fmaxf(a, b);
}
__device__ __forceinline__ void lds_arrive(LAS unsigned* c, int lane) {
    if (lane == 0) __hip_atomic_fetch_add(c, 1u, RLX_WG);
}
__device__ __forceinline__ void lds_wait(LAS unsigned* c, unsigned target) {
    asm volatile("" ::: "memory");
    for (unsigned spins = 0; spins < (1u << 22); ++spins) {
        if ((unsigned)__builtin_amdgcn_readfirstlane(__hip_atomic_load(c, RLX_WG)) >= target) break;
        __builtin_amdgcn_s_sleep(1);
    }
    asm volatile("" ::: "memory");
}

__global__ __launch_bounds__(256, 1) void k1_ln_qkv(const char* __restrict__ x_, const char* __restrict__ wqkv_, char* __restrict__ ws_) {
    __shared__ __attribute__((aligned(16))) char smem[3 * 512 * 64];
    const int lane = threadIdx.x & 63;
    const int w = __builtin_amdgcn_readfirstlane(threadIdx.x >> 6);
    const int b = blockIdx.x, xcd = b & 7, i = b >> 3;
    const int rb = (xcd >> 1) * 4 + (i >> 3), cg = (xcd & 1) * 8 + (i & 7);
    const int tt = rb * 4 + w;
    gcptr x = (gcptr)x_; gcptr wq = (gcptr)wqkv_; gptr ws = (gptr)ws_;
    const unsigned lds0 = (unsigned)(uintptr_t)(lptr)smem;

    if (b < 128 && threadIdx.x == 0) *(gu32*)(ws + WS_CNT + (size_t)b * 128) = 0u;

    u32x4 xa[16], xb[16];
    {
        gcptr xp = x + (size_t)(tt * 32 + (lane & 15)) * 1024 + (lane >> 4) * 16;
#pragma unroll
        for (int p = 0; p < 16; ++p) {
            xa[p] = *(GAS const u32x4*)(xp + p * 64);
            xb[p] = *(GAS const u32x4*)(xp + 16 * 1024 + p * 64);
        }
    }
    {
        const unsigned voff = (unsigned)((lane >> 2) * 3072 + (lane & 3) * 16);
#pragma unroll
        for (int j = 0; j < 8; ++j) {
            const int kb = j * 4 + w;
#pragma unroll
            for (int t = 0; t < 3; ++t)
                glds16(wq + (size_t)kb * 16 * 3072 + t * 1024 + cg * 64, voff, lds0 + t * 32768 + kb * 1024);
        }
    }
    float s1 = 0.f, s2 = 0.f;
#pragma unroll
    for (int p = 0; p < 16; ++p) {
#pragma unroll
        for (int d = 0; d < 4; ++d) {
            auto r = __builtin_amdgcn_permlane16_swap(xa[p][d], xb[p][d], false, false);
            xa[p][d] = r[0]; xb[p][d] = r[1];
            const float a0 = bf_lo(r[0]), a1 = bf_hi(r[0]), b0 = bf_lo(r[1]), b1 = bf_hi(r[1]);
            s1 += (a0 + a1) + (b0 + b1);
            s2 = __builtin_fmaf(a0, a0, s2); s2 = __builtin_fmaf(a1, a1, s2);
            s2 = __builtin_fmaf(b0, b0, s2); s2 = __builtin_fmaf(b1, b1, s2);
        }
    }
    s1 = half_swap_add(s1); s2 = half_swap_add(s2);
    const float mean = s1 * (1.f / 512.f);
    const float var = __builtin_fmaxf(s2 * (1.f / 512.f) - mean * mean, 0.f);
    const float mean_b = rbf(mean);
    const float sd = rbf(__builtin_sqrtf(rbf(rbf(var) + 1e-5f)));
    const float rs = 1.f / sd;
    const float nb = -mean_b * rs;

    asm volatile("s_waitcnt vmcnt(0)" ::: "memory");
    __syncthreads();

    f32x16 aq = {}, ak = {}, av = {};
    const lptr wb = (lptr)smem + ((lane >> 5) * 16 + ((lane & 15) >> 2)) * 64 + ((lane >> 4) & 1) * 32 + (lane & 3) * 8;
#pragma unroll
    for (int ks = 0; ks < 32; ++ks) {
        const u32x4 raw = (ks & 1) ? xb[ks >> 1] : xa[ks >> 1];
        u32x4 xn;
#pragma unroll
        for (int d = 0; d < 4; ++d)
            xn[d] = pk2(__builtin_fmaf(bf_lo(raw[d]), rs, nb), __builtin_fmaf(bf_hi(raw[d]), rs, nb));
        if ((ks >> 1) == cg) *(GAS u32x4*)(ws + WS_XN + (size_t)(tt * 32 + ks) * 1024 + lane * 16) = xn;
        const bf16x8 xf = __builtin_bit_cast(bf16x8, xn);
        const int ro = (32 * (ks >> 1) + 8 * (ks & 1)) * 64;
        const bf16x8 w0 = cat4(trd(wb + ro), trd(wb + ro + 256));
        const bf16x8 w1 = cat4(trd(wb + 32768 + ro), trd(wb + 32768 + ro + 256));
        const bf16x8 w2 = cat4(trd(wb + 65536 + ro), trd(wb + 65536 + ro + 256));
        aq = MFMA32(w0, xf, aq);
        ak = MFMA32(w1, xf, ak);
        av = MFMA32(xf, w2, av);
    }
    const float qs = 0.25f * 1.4426950408889634f;
    {
        const size_t o0 = (size_t)((2 * cg) * 64 + tt) * 1024 + lane * 16, o1 = o0 + 64 * 1024;
        *(GAS u32x4*)(ws + WS_Q + o0) = pack8(aq, 0, qs);
        *(GAS u32x4*)(ws + WS_Q + o1) = pack8(aq, 8, qs);
        *(GAS u32x4*)(ws + WS_K + o0) = pack8n(ak, 0);
        *(GAS u32x4*)(ws + WS_K + o1) = pack8n(ak, 8);
        const size_t ov = (size_t)((2 * cg + ((lane >> 4) & 1)) * 64 + tt) * 1024 + (lane >> 5) * 256 + (lane & 15) * 16;
        *(GAS u32x4*)(ws + WS_V + ov) = pack8n(av, 0);
        *(GAS u32x4*)(ws + WS_V + ov + 512) = pack8n(av, 8);
    }
}

constexpr int L2_RING = 0;
constexpr int L2_ONES = 32768;
constexpr int L2_CNT = L2_ONES + 4096;
constexpr int L2_W = L2_CNT + 256;
constexpr int GU_UP = 14336 + 128;
constexpr int GU_STAGE = 28928;
constexpr int L2_BYTES = L2_W + 4 * GU_STAGE;

__device__ __forceinline__ void attn_slow(gcptr kf, gcptr vf, bf16x8 q, int lane, f32x16& o) {
    float m = -__builtin_inff();
    o = f32x16{};
    const bool data = (lane & 16) == 0;
    const u32x4 ones = {0x3f803f80u, 0x3f803f80u, 0x3f803f80u, 0x3f803f80u};
    for (int t = 0; t < 64; ++t) {
        const bf16x8 k = *(GAS const bf16x8*)(kf + t * 1024 + lane * 16);
        u32x4 v0 = *(GAS const u32x4*)(vf + t * 1024 + (lane >> 5) * 256 + (lane & 15) * 16);
        u32x4 v1 = *(GAS const u32x4*)(vf + t * 1024 + 512 + (lane >> 5) * 256 + (lane & 15) * 16);
        if (!data) { v0 = ones; v1 = ones; }
        f32x16 s = MFMA32(k, q, f32x16{});
        const float mn = __builtin_fmaxf(m, half_swap_max(max16(s)));
        const float al = __builtin_amdgcn_exp2f(m - mn);
        m = mn;
#pragma unroll
        for (int r = 0; r < 16; ++r) { s[r] = __builtin_amdgcn_exp2f(s[r] - mn); o[r] *= al; }
        o = MFMA32(__builtin_bit_cast(bf16x8, v0), __builtin_bit_cast(bf16x8, pack8n(s, 0)), o);
        o = MFMA32(__builtin_bit_cast(bf16x8, v1), __builtin_bit_cast(bf16x8, pack8n(s, 8)), o);
    }
}

__device__ __forceinline__ void attn_wave(gptr ws, lptr smem, int b, int w, int lane) {
    const int xcd = b & 7, i = b >> 3;
    const int head = xcd * 4 + (i >> 3), tt = (i & 7) * 8 + w;
    const unsigned lds0 = (unsigned)(uintptr_t)smem;
    LAS unsigned* cnt = (LAS unsigned*)(smem + L2_CNT);
    gcptr kf = (gcptr)ws + WS_K + (size_t)head * 65536;
    gcptr vf = (gcptr)ws + WS_V + (size_t)head * 65536;
    gcptr src = (w < 4 ? kf : vf) + (w & 3) * 1024;
    const unsigned voff = lane * 16;
    const unsigned dst = lds0 + L2_RING + w * 1024;
    glds16(src, voff, dst);
    glds16(src + 4096, voff, dst + 8192);
    const bf16x8 q = *(GAS const bf16x8*)((gcptr)ws + WS_Q + (size_t)(head * 64 + tt) * 1024 + lane * 16);
    f32x16 cinit;
    {
        const bf16x8 k0 = *(GAS const bf16x8*)(kf + lane * 16);
        const f32x16 s = MFMA32(k0, q, f32x16{});
        const float nm = -half_swap_max(max16(s));
#pragma unroll
        for (int r = 0; r < 16; ++r) cinit[r] = nm;
    }
    asm volatile("s_waitcnt vmcnt(0)" ::: "memory");
    lds_arrive(cnt, lane);
    const lptr kb = smem + L2_RING + lane * 16;
    const bool data = (lane & 16) == 0;
    const unsigned vdat = L2_RING + 4096 + (lane >> 5) * 256 + (lane & 15) * 16;
    const unsigned vone = L2_ONES + ((((lane & 15) + 12) & 8) ? 0 : 64);
    f32x16 o = {};
    for (int c = 0; c < 16; ++c) {
        lds_wait(cnt, 8u * (unsigned)(c + 1));
        if (c > 0) { asm volatile("s_waitcnt vmcnt(0) lgkmcnt(0)" ::: "memory"); }
        lds_arrive(cnt, lane);
        if (c + 2 < 16) glds16(src + (size_t)(c + 2) * 4096, voff, dst + ((c + 2) & 3) * 8192);
        const unsigned so = (c & 3) * 8192;
        const lptr kp = kb + so;
        const lptr vp = smem + (data ? vdat + so : vone);
#pragma unroll
        for (int t = 0; t < 4; ++t) {
            const bf16x8 k = *(LAS const bf16x8*)(kp + t * 1024);
            const bf16x8 v0 = *(LAS const bf16x8*)(vp + t * 1024);
            const bf16x8 v1 = *(LAS const bf16x8*)(vp + t * 1024 + 512);
            f32x16 s = MFMA32(k, q, cinit);
#pragma unroll
            for (int r = 0; r < 16; ++r) s[r] = __builtin_amdgcn_exp2f(s[r]);
            o = MFMA32(v0, __builtin_bit_cast(bf16x8, pack8n(s, 0)), o);
            o = MFMA32(v1, __builtin_bit_cast(bf16x8, pack8n(s, 8)), o);
        }
    }
    bool ok = o[8] <= 1.0995116e12f;
#pragma unroll
    for (int r = 0; r < 8; ++r) ok = ok && (__builtin_fabsf(o[r]) <= 3.0e38f);
    if (__builtin_expect(!__all(ok), 0)) attn_slow(kf, vf, q, lane, o);
    const float rl = 1.f / o[8];
    *(GAS u32x4*)(ws + WS_O + (size_t)(tt * 32 + head) * 1024 + lane * 16) = pack8(o, 0, rl);
}

__device__ __forceinline__ void ffn_wave(gcptr wg, gcptr wu, gcptr wd, gptr ws, lptr smem, int b, int fw, int lane) {
    const int xcd = b & 7, i = b >> 3;
    const int fh = xcd & 1, rb = (xcd >> 1) * 4 + (i >> 3), j8 = i & 7;
    const int fb = fh * 8 + j8, tt = rb * 4 + fw;
    const unsigned lds0 = (unsigned)(uintptr_t)smem;
    LAS unsigned* cnt = (LAS unsigned*)(smem + L2_CNT + 64);
    unsigned voff[7];
#pragma unroll
    for (int p = 0; p < 7; ++p) { const int c = p * 64 + lane; voff[p] = (unsigned)((c / 14) * 3584 + (c % 14) * 16); }
    gcptr wsrc = ((fw >> 1) ? wu : wg) + (size_t)((fw & 1) * 32) * 3584 + fb * 224;
    const unsigned wdst = lds0 + L2_W + (fw >> 1) * GU_UP + (fw & 1) * 7168;
#define GU_LOAD(s) do { _Pragma("unroll") for (int p = 0; p < 7; ++p) glds16(wsrc + (size_t)(s) * 64 * 3584, voff[p], wdst + ((s) & 3) * GU_STAGE + p * 1024); } while (0)
    GU_LOAD(0); GU_LOAD(1);
    gcptr xp = (gcptr)ws + WS_XN + (size_t)tt * 32768 + lane * 16;
    bf16x8 xf[4];
#pragma unroll
    for (int k = 0; k < 4; ++k) xf[k] = *(GAS const bf16x8*)(xp + k * 1024);
    f32x16 acc[7];
#pragma unroll
    for (int f = 0; f < 7; ++f) acc[f] = f32x16{};
    asm volatile("s_waitcnt vmcnt(0)" ::: "memory");
    lds_arrive(cnt, lane);
    const lptr wb = smem + L2_W + ((lane >> 4) & 1) * GU_UP + ((lane >> 5) * 16 + ((lane & 15) >> 2)) * 224 + (lane & 3) * 8;
#pragma unroll 2
    for (int s = 0; s < 8; ++s) {
        lds_wait(cnt, 4u * (unsigned)(s + 1));
        if (s > 0) { asm volatile("s_waitcnt vmcnt(4) lgkmcnt(0)" ::: "memory"); }
        lds_arrive(cnt, lane);
        if (s + 2 < 8) GU_LOAD(s + 2);
        const lptr wp = wb + (s & 3) * GU_STAGE;
#pragma unroll
        for (int k = 0; k < 4; ++k) {
            const bf16x8 xc = xf[k];
            const int ro = (32 * (k >> 1) + 8 * (k & 1)) * 224;
#pragma unroll
            for (int f = 0; f < 7; ++f) {
                const bf16x8 wf = cat4(trd(wp + ro + f * 32), trd(wp + ro + 4 * 224 + f * 32));
                acc[f] = MFMA32(wf, xc, acc[f]);
            }
            const int kn = (s * 4 + k + 4) & 31;
            xf[k] = *(GAS const bf16x8*)(xp + kn * 1024);
        }
    }
#undef GU_LOAD
    asm volatile("s_waitcnt lgkmcnt(0)" ::: "memory");
    lds_arrive(cnt, lane);
    lds_wait(cnt, 40u);
    {
        const unsigned vo = (unsigned)((lane >> 2) * 1024 + (lane & 3) * 16);
        gcptr dsrc = wd + (size_t)(fh * 896) * 1024 + j8 * 128;
#pragma unroll
        for (int j = 0; j < 14; ++j) {
            const int kb = j * 4 + fw;
            glds16(dsrc + (size_t)kb * 16384, vo, lds0 + L2_W + kb * 1024);
            glds16(dsrc + (size_t)kb * 16384 + 64, vo, lds0 + L2_W + 57344 + kb * 1024);
        }
    }
    {
        const __amdgpu_buffer_rsrc_t hr = __builtin_amdgcn_make_buffer_rsrc((void*)(ws + WS_H), 0, (int)H_BYTES, 0x00020000);
#pragma unroll
        for (int f = 0; f < 7; ++f) {
            float hv[8];
#pragma unroll
            for (int j = 0; j < 8; ++j) {
                const float e = __builtin_amdgcn_exp2f(acc[f][j] * -1.4426950408889634f);
                hv[j] = acc[f][8 + j] * __builtin_amdgcn_rcpf(1.f + e);
            }
            const u32x4 pv = {pk2(hv[0], hv[1]), pk2(hv[2], hv[3]), pk2(hv[4], hv[5]), pk2(hv[6], hv[7])};
            __builtin_amdgcn_raw_buffer_store_b128(pv, hr, (unsigned)((tt * 112 + fb * 7 + f) * 1024 + lane * 16), 0, 16);
        }
    }
    asm volatile("s_waitcnt vmcnt(0)" ::: "memory");
    gu32* gc = (gu32*)(ws + WS_CNT + (size_t)(tt * 2 + fh) * 128);
    if (lane == 0) __hip_atomic_fetch_add(gc, 1u, RLX_AGENT);
    lds_arrive(cnt, lane);
    for (unsigned spins = 0; spins < (1u << 22); ++spins) {
        if ((unsigned)__builtin_amdgcn_readfirstlane(__hip_atomic_load(gc, RLX_AGENT)) >= 8u) break;
        __builtin_amdgcn_s_sleep(2);
    }
    __builtin_amdgcn_fence(__ATOMIC_ACQUIRE, "agent");
    lds_wait(cnt, 44u);
    gcptr hp = (gcptr)ws + WS_H + (size_t)(tt * 112 + fh * 56) * 1024 + lane * 16;
    const lptr db = smem + L2_W + ((lane >> 5) * 4 + ((lane & 15) >> 2)) * 64 + ((lane >> 4) & 1) * 32 + (lane & 3) * 8;
    f32x16 d0 = {}, d1 = {};
#pragma unroll
    for (int hb = 0; hb < 2; ++hb) {
        bf16x8 hf[28];
#pragma unroll
        for (int k = 0; k < 28; ++k) hf[k] = *(GAS const bf16x8*)(hp + (hb * 28 + k) * 1024);
#pragma unroll
        for (int k = 0; k < 28; ++k) {
            const int ro = (hb * 28 + k) * 1024;
            const bf16x8 w0 = cat4(trd(db + ro), trd(db + ro + 512));
            const bf16x8 w1 = cat4(trd(db + 57344 + ro), trd(db + 57344 + ro + 512));
            d0 = MFMA32(w0, hf[k], d0);
            d1 = MFMA32(w1, hf[k], d1);
        }
    }
    gptr dp = ws + WS_DP + (size_t)((fh * 64 + tt) * 16 + j8 * 2) * 2048 + lane * 16;
    *(GAS u32x4*)(dp) = pack8n(d0, 0);
    *(GAS u32x4*)(dp + 1024) = pack8n(d0, 8);
    *(GAS u32x4*)(dp + 2048) = pack8n(d1, 0);
    *(GAS u32x4*)(dp + 3072) = pack8n(d1, 8);
}

__global__ __launch_bounds__(768, 3) void k2_attn_ffn(const char* __restrict__ wg, const char* __restrict__ wu, const char* __restrict__ wd, char* ws) {
    __shared__ __attribute__((aligned(16))) char smem[L2_BYTES];
    const int lane = threadIdx.x & 63;
    const int w = __builtin_amdgcn_readfirstlane(threadIdx.x >> 6);
    if (threadIdx.x < 256) *(LAS u32x4*)((lptr)smem + L2_ONES + threadIdx.x * 16) = u32x4{0x3f803f80u, 0x3f803f80u, 0x3f803f80u, 0x3f803f80u};
    else if (threadIdx.x < 320) *(LAS unsigned*)((lptr)smem + L2_CNT + (threadIdx.x - 256) * 4) = 0u;
    __syncthreads();
    if (w < 8) attn_wave((gptr)ws, (lptr)smem, blockIdx.x, w, lane);
    else ffn_wave((gcptr)wg, (gcptr)wu, (gcptr)wd, (gptr)ws, (lptr)smem, blockIdx.x, w - 8, lane);
}

__global__ __launch_bounds__(256, 1) void k3_out(const char* __restrict__ x_, const char* __restrict__ wo_, const char* __restrict__ ws_, char* __restrict__ out_) {
    __shared__ __attribute__((aligned(16))) char smem[65536 + 16384];
    const int lane = threadIdx.x & 63;
    const int w = __builtin_amdgcn_readfirstlane(threadIdx.x >> 6);
    const int b = blockIdx.x, xcd = b & 7, i = b >> 3;
    const int tb = xcd * 4 + (i >> 3), cb = i & 7;
    const int tt = tb * 2 + (w & 1), kh = w >> 1;
    gcptr ws = (gcptr)ws_; gcptr wo = (gcptr)wo_;
    const unsigned lds0 = (unsigned)(uintptr_t)(lptr)smem;
    {
        const unsigned vo = (unsigned)((lane >> 2) * 1024 + (lane & 3) * 16);
        gcptr src = wo + cb * 128;
#pragma unroll
        for (int j = 0; j < 8; ++j) {
            const int kb = j * 4 + w;
            glds16(src + (size_t)kb * 16384, vo, lds0 + kb * 1024);
            glds16(src + (size_t)kb * 16384 + 64, vo, lds0 + 32768 + kb * 1024);
        }
    }
    bf16x8 of[16];
    {
        gcptr op = ws + WS_O + (size_t)(tt * 32 + kh * 16) * 1024 + lane * 16;
#pragma unroll
        for (int k = 0; k < 16; ++k) of[k] = *(GAS const bf16x8*)(op + k * 1024);
    }
    const int ct = cb * 2 + kh;
    u32x4 dpa[2], dpb[2];
    {
        gcptr dp = ws + WS_DP + (size_t)(tt * 16 + ct) * 2048 + lane * 16;
        dpa[0] = *(GAS const u32x4*)(dp); dpa[1] = *(GAS const u32x4*)(dp + 1024);
        dpb[0] = *(GAS const u32x4*)(dp + (2u << 20)); dpb[1] = *(GAS const u32x4*)(dp + (2u << 20) + 1024);
    }
    const size_t xo = (size_t)(tt * 32 + (lane & 31)) * 1024 + ct * 64 + (lane >> 5) * 8;
    u32x2 xv[4];
#pragma unroll
    for (int g = 0; g < 4; ++g) xv[g] = *(GAS const u32x2*)((gcptr)x_ + xo + g * 16);

    asm volatile("s_waitcnt vmcnt(0)" ::: "memory");
    __syncthreads();
    const lptr wb = (lptr)smem + kh * 16384 + ((lane >> 5) * 4 + ((lane & 15) >> 2)) * 64 + ((lane >> 4) & 1) * 32 + (lane & 3) * 8;
    f32x16 a0 = {}, a1 = {};
#pragma unroll
    for (int k = 0; k < 16; ++k) {
        const bf16x8 w0 = cat4(trd(wb + k * 1024), trd(wb + k * 1024 + 512));
        const bf16x8 w1 = cat4(trd(wb + 32768 + k * 1024), trd(wb + 32768 + k * 1024 + 512));
        a0 = MFMA32(w0, of[k], a0);
        a1 = MFMA32(w1, of[k], a1);
    }
    typedef float f32x4 __attribute__((ext_vector_type(4)));
    {
        const f32x16 snd = kh ? a0 : a1;
        const lptr ex = (lptr)smem + 65536 + w * 4096 + lane * 16;
#pragma unroll
        for (int g = 0; g < 4; ++g) *(LAS f32x4*)(ex + g * 1024) = f32x4{snd[4 * g], snd[4 * g + 1], snd[4 * g + 2], snd[4 * g + 3]};
    }
    __syncthreads();
    f32x16 s = kh ? a1 : a0;
    {
        const lptr ex = (lptr)smem + 65536 + (w ^ 2) * 4096 + lane * 16;
#pragma unroll
        for (int g = 0; g < 4; ++g) { const f32x4 v = *(LAS const f32x4*)(ex + g * 1024); s[4 * g] += v[0]; s[4 * g + 1] += v[1]; s[4 * g + 2] += v[2]; s[4 * g + 3] += v[3]; }
    }
#pragma unroll
    for (int g = 0; g < 4; ++g) {
        u32x2 ov;
#pragma unroll
        for (int e = 0; e < 2; ++e) {
            const unsigned pa = dpa[g >> 1][(g & 1) * 2 + e], pb = dpb[g >> 1][(g & 1) * 2 + e];
            const float d0 = bf_lo(pa) + bf_lo(pb), d1 = bf_hi(pa) + bf_hi(pb);
            const float g0 = d0 * __builtin_amdgcn_rcpf(1.f + __builtin_amdgcn_exp2f(d0 * -1.4426950408889634f));
            const float g1 = d1 * __builtin_amdgcn_rcpf(1.f + __builtin_amdgcn_exp2f(d1 * -1.4426950408889634f));
            ov[e] = pk2(__builtin_fmaf(s[4 * g + 2 * e], g0, bf_lo(xv[g][e])), __builtin_fmaf(s[4 * g + 2 * e + 1], g1, bf_hi(xv[g][e])));
        }
        *(GAS u32x2*)((gptr)out_ + xo + g * 16) = ov;
    }
}

extern "C" void kernel_launch(void* const* d_in, const int* in_sizes, int n_in,
                              void* d_out, int out_size, void* d_ws, size_t ws_size,
                              hipStream_t stream) {
    const char* x = (const char*)d_in[0];
    const char* wqkv = (const char*)d_in[1];
    const char* wo = (const char*)d_in[2];
    const char* wg = (const char*)d_in[4];
    const char* wu = (const char*)d_in[5];
    const char* wd = (const char*)d_in[6];
    char* ws = (char*)d_ws;
    k1_ln_qkv<<<256, 256, 0, stream>>>(x, wqkv, ws);
    k2_attn_ffn<<<256, 768, 0, stream>>>(wg, wu, wd, ws);
    k3_out<<<256, 256, 0, stream>>>(x, wo, ws, (char*)d_out);
}
```

```cpp
#include <hip/hip_runtime.h>
#include <hip/hip_bf16.h>
#include <stdint.h>

typedef short bf16x8 __attribute__((ext_vector_type(8)));
typedef short s16x4 __attribute__((ext_vector_type(4)));
typedef float f32x16 __attribute__((ext_vector_type(16)));
typedef float f32x4 __attribute__((ext_vector_type(4)));
typedef float f32x2 __attribute__((ext_vector_type(2)));
typedef __bf16 bfx2 __attribute__((ext_vector_type(2)));
typedef unsigned u32x4 __attribute__((ext_vector_type(4)));
typedef unsigned u32x2 __attribute__((ext_vector_type(2)));
typedef unsigned long long u64;
#define LAS __attribute__((address_space(3)))
#define GAS __attribute__((address_space(1)))
typedef LAS char* lptr;
typedef GAS const char* gcptr;
typedef GAS char* gptr;
typedef GAS u64 gu64;

#define MFMA32(a, b, c) __builtin_amdgcn_mfma_f32_32x32x16_bf16((a), (b), (c), 0, 0, 0)
#define RLX_AGENT __ATOMIC_RELAXED, __HIP_MEMORY_SCOPE_AGENT
#define RLX_WG __ATOMIC_RELAXED, __HIP_MEMORY_SCOPE_WORKGROUP
#define PB(x) __builtin_bit_cast(bf16x8, x)

constexpr unsigned WS_Q = 2u << 20;
constexpr unsigned WS_K = 4u << 20;
constexpr unsigned WS_V = 6u << 20;
constexpr unsigned WS_O = 8u << 20;
constexpr unsigned WS_H = 14u << 20;
constexpr unsigned WS_N = 22u << 20;
constexpr unsigned WS_F1 = WS_N + 4096;
constexpr unsigned WS_F2 = WS_N + 8192;
constexpr unsigned WS_F3 = WS_N + 12288;
constexpr unsigned WS_BYTES = 24u << 20;

constexpr int L_WQ0 = 131584;
constexpr int L_MISC = 156160;
constexpr int L_ONES = L_MISC;
constexpr int L_DUMP = L_MISC + 2048;
constexpr int L_CSQ = L_MISC + 2304;
constexpr int L_STAT = L_MISC + 5376;
constexpr int L_CNT = L_MISC + 6400;
constexpr int L_SCR = 144640;
constexpr int GU_UP = 7168 + 128;
constexpr int GU_SLOT = 14464;
constexpr int GU_PAIR = 2 * GU_SLOT;
constexpr int L_COMB = 131072;

#define STAMP(n) do {} while (0)
#define SB() __builtin_amdgcn_sched_barrier(0)
#define PIN(x) asm volatile("" : "+v"(x))
#define BAR() asm volatile("s_barrier" ::: "memory")
#define WAITV(n) asm volatile("s_waitcnt vmcnt(" #n ")" ::: "memory")
#define WAITL() asm volatile("s_waitcnt lgkmcnt(0)" ::: "memory")
__device__ __forceinline__ unsigned pk2(float lo, float hi) {
    f32x2 v = {lo, hi};
    return __builtin_bit_cast(unsigned, __builtin_convertvector(v, bfx2));
}
__device__ __forceinline__ float bf_lo(unsigned u) { return __uint_as_float(u << 16); }
__device__ __forceinline__ float bf_hi(unsigned u) { return __uint_as_float(u & 0xffff0000u); }
__device__ __forceinline__ float rbf(float f) { return bf_lo(pk2(f, f) & 0xffffu); }
__device__ __forceinline__ void glds16(gcptr sbase, unsigned voff, unsigned lds_base) {
    asm volatile("s_mov_b32 m0, %2\n\ts_nop 0\n\tglobal_load_lds_dwordx4 %0, %1" :: "v"(voff), "s"(sbase), "s"(lds_base) : "memory");
}
__device__ __forceinline__ void glds16_dev(gcptr sbase, unsigned voff, unsigned lds_base) {
    asm volatile("s_mov_b32 m0, %2\n\ts_nop 0\n\tglobal_load_lds_dwordx4 %0, %1 sc1" :: "v"(voff), "s"(sbase), "s"(lds_base) : "memory");
}
__device__ __forceinline__ void touch(gcptr sbase, unsigned voff, unsigned lds_dump) {
    asm volatile("s_mov_b32 m0, %2\n\ts_nop 0\n\tglobal_load_lds_dword %0, %1" :: "v"(voff), "s"(sbase), "s"(lds_dump) : "memory");
}
__device__ __forceinline__ void gload16_dev(u32x4& d, gcptr sbase, unsigned voff) {
    asm volatile("global_load_dwordx4 %0, %1, %2 sc1" : "=v"(d) : "v"(voff), "s"(sbase) : "memory");
}
__device__ __forceinline__ void gload16(u32x4& d, gcptr sbase, unsigned voff) {
    asm volatile("global_load_dwordx4 %0, %1, %2" : "=v"(d) : "v"(voff), "s"(sbase) : "memory");
}
__device__ __forceinline__ void gload8_dev(u64& d, gcptr sbase, unsigned voff) {
    asm volatile("global_load_dwordx2 %0, %1, %2 sc1" : "=v"(d) : "v"(voff), "s"(sbase) : "memory");
}
__device__ __forceinline__ s16x4 trd(lptr p) { return __builtin_amdgcn_ds_read_tr16_b64_v4i16((LAS s16x4*)p); }
__device__ __forceinline__ bf16x8 cat4(s16x4 a, s16x4 b) { return __builtin_shufflevector(a, b, 0, 1, 2, 3, 4, 5, 6, 7); }
__device__ __forceinline__ u32x4 pack8(const f32x16& a, int o, float s) {
    u32x4 r;
    r[0] = pk2(a[o + 0] * s, a[o + 1] * s); r[1] = pk2(a[o + 2] * s, a[o + 3] * s);
    r[2] = pk2(a[o + 4] * s, a[o + 5] * s); r[3] = pk2(a[o + 6] * s, a[o + 7] * s);
    return r;
}
__device__ __forceinline__ u32x4 pack8n(const f32x16& a, int o) {
    u32x4 r;
    r[0] = pk2(a[o + 0], a[o + 1]); r[1] = pk2(a[o + 2], a[o + 3]);
    r[2] = pk2(a[o + 4], a[o + 5]); r[3] = pk2(a[o + 6], a[o + 7]);
    return r;
}
__device__ __forceinline__ float half_swap_add(float v) {
    auto r = __builtin_amdgcn_permlane32_swap(__float_as_uint(v), __float_as_uint(v), false, false);
    return __uint_as_float(r[0]) + __uint_as_float(r[1]);
}
__device__ __forceinline__ float half_swap_max(float v) {
    auto r = __builtin_amdgcn_permlane32_swap(__float_as_uint(v), __float_as_uint(v), false, false);
    return __builtin_fmaxf(__uint_as_float(r[0]), __uint_as_float(r[1]));
}
__device__ __forceinline__ float max16(const f32x16& s) {
    float a = __builtin_fmaxf(__builtin_fmaxf(s[0], s[1]), s[2]), b = __builtin_fmaxf(__builtin_fmaxf(s[3], s[4]), s[5]);
    a = __builtin_fmaxf(__builtin_fmaxf(a, s[6]), s[7]);   b = __builtin_fmaxf(__builtin_fmaxf(b, s[8]), s[9]);
    a = __builtin_fmaxf(__builtin_fmaxf(a, s[10]), s[11]); b = __builtin_fmaxf(__builtin_fmaxf(b, s[12]), s[13]);
    a = __builtin_fmaxf(__builtin_fmaxf(a, s[14]), s[15]);
    return __builtin_fmaxf(a, b);
}
__device__ __forceinline__ lptr lbase(lptr p) { unsigned u = (unsigned)(uintptr_t)p; asm volatile("" : "+v"(u)); return (lptr)(uintptr_t)u; }
__device__ __forceinline__ float dot2(unsigned a, unsigned b, float c) {
    return __builtin_amdgcn_fdot2_f32_bf16(__builtin_bit_cast(bfx2, a), __builtin_bit_cast(bfx2, b), c, false);
}
__device__ __forceinline__ float sum8(bf16x8 f, float c) {
    const u32x4 u = __builtin_bit_cast(u32x4, f);
    c = dot2(u[0], 0x3f803f80u, c); c = dot2(u[1], 0x3f803f80u, c); c = dot2(u[2], 0x3f803f80u, c); return dot2(u[3], 0x3f803f80u, c);
}
__device__ __forceinline__ unsigned split_hl(float v) {
    const float hi = rbf(v);
    return pk2(hi, v - hi);
}
__device__ __forceinline__ void poll_flags(gcptr base, unsigned voff, u64 epoch) {
#pragma nounroll
    for (unsigned spins = 0; spins < (1u << 20); ++spins) {
        const u64 v = __hip_atomic_load((GAS const u64*)(base + voff), RLX_AGENT);
        if (__all(v == epoch)) break;
        __builtin_amdgcn_s_sleep(2);
    }
    asm volatile("" ::: "memory");
}

template <int V> struct IC { static constexpr int value = V; };
template <int I, int N, class F> __device__ __forceinline__ void static_for(F&& f) {
    if constexpr (I < N) { f(IC<I>{}); static_for<I + 1, N>(f); }
}
#define SFOR(var, N) static_for<0, (N)>([&](auto ic_##var) __attribute__((always_inline)) { constexpr int var = decltype(ic_##var)::value;
#define SEND });

struct Ctx {
    gcptr x, wq, wo, wg, wu, wd; gptr ws; lptr smem; unsigned lds0; u64 epoch;
    int lane, w, xcd, i;
};

template <int G>
__device__ __forceinline__ void front(Ctx& c, u64 ep, const __amdgpu_buffer_rsrc_t rs_ws, u32x4& qa, u32x4& qb, u32x4& k0, u32x4& kc) {
    const int lane = c.lane, w = c.w, tau = w & 3, xcd = c.xcd, i = c.i;
    const int rb = (xcd >> 1) * 4 + (i >> 3), j = (xcd & 1) * 8 + (i & 7), tt = rb * 4 + tau;
    const lptr smem = c.smem; const unsigned lds0 = c.lds0; gptr ws = c.ws;
    const unsigned lin = lane * 16;
    unsigned vq[3], vg[7];
#pragma unroll
    for (int m = 0; m < 3; ++m) { const int e = m * 64 + lane; vq[m] = (unsigned)((e / 12) * 3072 + (e % 12) * 16); }
    gcptr wsrc = c.wq + j * 192 + (size_t)w * 16 * 3072;
#define WQ_LOAD(ch) do { _Pragma("unroll") for (int m = 0; m < 3; ++m) glds16(wsrc + (size_t)(ch) * 128 * 3072, vq[m], lds0 + L_WQ0 - (ch) * 24576 + w * 3072 + m * 1024); } while (0)
    {
        const unsigned v0 = (unsigned)((lane >> 3) * 1024 + (((lane & 7) ^ (lane >> 4)) * 16));
        const unsigned v1 = (unsigned)((8 + (lane >> 3)) * 1024 + (((lane & 7) ^ (4 + (lane >> 4))) * 16));
        gcptr xp = c.x + (size_t)(tt * 32 + G * 16) * 1024;
        const unsigned xd = lds0 + tau * 4096 + G * 2048;
#pragma unroll
        for (int u = 0; u < 8; ++u) {
            if (u == 4) WQ_LOAD(0);
            glds16(xp + u * 128, v0, xd + (7 - u) * 16384); glds16(xp + u * 128, v1, xd + (7 - u) * 16384 + 1024);
        }
    }
    if (w < 2) {
        const unsigned o = (lane & 1) ? 0u : 0x3f803f80u;
        *(LAS u32x4*)(smem + L_ONES + w * 1024 + lin) = u32x4{o, o, o, o};
    } else if (w == 2) *(LAS unsigned*)(smem + L_CNT + lane * 4) = 0u;
#pragma unroll
    for (int p = 0; p < 7; ++p) { const int e = p * 64 + lane; vg[p] = (unsigned)((e / 14) * 3584 + (e % 14) * 16); }
    gcptr gsrc = ((tau >> 1) ? c.wu : c.wg) + (size_t)((tau & 1) * 32) * 3584 + j * 224;
    const unsigned gdst = lds0 + (tau >> 1) * GU_UP + (tau & 1) * GU_SLOT;
#define GU_PS(D) ((D) == 0 || (D) == 5 ? 0 : (D) == 1 || (D) == 6 ? 1 : (D) == 2 || (D) == 7 ? 4 : (D) == 3 ? 3 : 2)
#define GU_LOAD(D) do { _Pragma("unroll") for (int p = 0; p < 7; ++p) glds16(gsrc + (size_t)(D) * 64 * 3584, vg[p], gdst + GU_PS(D) * GU_PAIR + p * 1024); } while (0)
    u32x4 xr[32];
    lptr xb[4];
#pragma unroll
    for (int q = 0; q < 4; ++q)
        xb[q] = lbase(smem + 65536 + tau * 4096 + (lane & 31) * 128 + (((2 * q + (lane >> 5)) ^ ((lane >> 1) & 7)) * 16));
    const lptr wl = smem + ((lane >> 5) * 8 + ((lane & 15) >> 2)) * 192 + ((lane >> 4) & 1) * 32 + (lane & 3) * 8 + G * 128;
    lptr wb[2];
    wb[0] = lbase(wl + L_WQ0 - 24576); wb[1] = lbase(wl + L_WQ0 - 3 * 24576);
    WAITV(8); BAR();
    PIN(ep);
    c.epoch = (((u64)(unsigned)__builtin_amdgcn_readfirstlane((unsigned)(ep >> 32)) << 32) | (unsigned)__builtin_amdgcn_readfirstlane((unsigned)ep)) + 1ull;
    STAMP(1);
#pragma unroll
    for (int ks = 0; ks < 16; ++ks) xr[ks] = *(LAS const u32x4*)(xb[ks & 3] + (3 - (ks >> 2)) * 16384);
    constexpr int WD = 4;
    constexpr int NT = G ? 1 : 2;
    bf16x8 wf[WD];
#define Q_OFF(n) ((1 - ((((n) / NT) >> 3) & 1)) * 24576 + (((n) / NT) & 7) * 16 * 192 + ((n) % NT) * 64)
#define Q_FRAG(n) cat4(trd(wb[(n) / (16 * NT)] + Q_OFF(n)), trd(wb[(n) / (16 * NT)] + Q_OFF(n) + 768))
#pragma unroll
    for (int m = 0; m < WD; ++m) wf[m] = Q_FRAG(m);
    WAITL(); BAR();
    WQ_LOAD(1); WQ_LOAD(2);
    {
        int L = (w >> 1) * 2048 + i * 64 + lane; if (L > 7167) L = 7167;
        touch(((w & 1) ? c.wu : c.wg) + (xcd & 1) * 1792, (unsigned)((L / 14) * 3584 + (L % 14) * 128), lds0 + L_DUMP);
    }
    f32x16 aq[NT] = {};
    float cq[NT] = {};
    float s1 = 0.f, s2 = 0.f;
    SFOR(n, 32 * NT)
        constexpr int ks = n / NT, t = n % NT;
        if (n == 8 * NT - WD) {
            STAMP(16); WAITV(4); BAR(); STAMP(17);
#pragma unroll
            for (int q = 0; q < 4; ++q) xb[q] = lbase(xb[q] - 65536);
#pragma unroll
            for (int k2 = 16; k2 < 32; ++k2) xr[k2] = *(LAS const u32x4*)(xb[k2 & 3] + (7 - (k2 >> 2)) * 16384);
        }
        if (n == 16 * NT - WD) {
            STAMP(18);
            asm volatile("s_waitcnt vmcnt(1) lgkmcnt(0)\n\ts_barrier" ::: "memory");
            STAMP(19);
            WQ_LOAD(3); GU_LOAD(G);
        }
        if (n == 24 * NT - WD) { STAMP(20); WAITV(7); BAR(); STAMP(21); if (G == 0) GU_LOAD(2); }
        aq[t] = MFMA32(wf[n % WD], PB(xr[ks]), aq[t]);
        PIN(aq[t]);
        if ((ks & 3) == tau) { cq[t] = sum8(wf[n % WD], cq[t]); PIN(cq[t]); }
        if constexpr (n + WD < 32 * NT) wf[n % WD] = Q_FRAG(n + WD);
        if (G) {
#pragma unroll
            for (int d = 0; d < 4; ++d) { s1 = dot2(xr[ks][d], 0x3f803f80u, s1); s2 = dot2(xr[ks][d], xr[ks][d], s2); }
            PIN(s1); PIN(s2);
        }
        SB();
    SEND
#undef Q_FRAG
#undef Q_OFF
#undef WQ_LOAD
    STAMP(22);
#pragma unroll
    for (int t = 0; t < NT; ++t) *(LAS unsigned*)(smem + L_CSQ + (G * 2 + t) * 1024 + lin + tau * 4) = split_hl(cq[t]);
    float mean_b, sd;
    if (G) {
        const float t1 = half_swap_add(s1), t2 = half_swap_add(s2);
        const float mean = t1 * (1.f / 512.f);
        const float var = __builtin_fmaxf(t2 * (1.f / 512.f) - mean * mean, 0.f);
        mean_b = rbf(mean);
        sd = rbf(__builtin_sqrtf(rbf(rbf(var) + 1e-5f)));
        *(LAS f32x2*)(smem + L_STAT + tau * 256 + (lane & 31) * 8) = f32x2{mean_b, sd};
        asm volatile("s_waitcnt lgkmcnt(0)\n\ts_barrier" ::: "memory");
    } else {
        asm volatile("s_waitcnt vmcnt(7) lgkmcnt(0)\n\ts_barrier" ::: "memory");
        const f32x2 st = *(LAS const f32x2*)(smem + L_STAT + tau * 256 + (lane & 31) * 8);
        mean_b = st[0]; sd = st[1];
    }
    STAMP(2);
    GU_LOAD(4 - G);
    const float rs = 1.f / sd;
    const unsigned nm = pk2(-mean_b, -mean_b);
    const bf16x8 muf = PB((u32x4{nm, nm, nm, nm}));
    {
        const lptr tw = smem + L_SCR + w * 1024 + ((lane & 31) >> 2) * 128 + (lane >> 5) * 64 + (lane & 3) * 16;
        const lptr tr = smem + L_SCR + w * 1024 + ((lane >> 4) & 1) * 512 + (lane >> 5) * 128 + ((lane >> 1) & 1) * 64 + ((lane & 15) >> 2) * 16 + (lane & 1) * 8;
        const unsigned vo = (unsigned)(((lane >> 4) & 1) * 512 + (lane >> 5) * 256 + (lane & 15) * 16);
#pragma unroll
        for (int t = 0; t < NT; ++t) {
            const bf16x8 cf = *(LAS const bf16x8*)(smem + L_CSQ + (G * 2 + t) * 1024 + lin);
            const f32x16 a = MFMA32(cf, muf, aq[t]);
            const int idx = 3 * j + G * 2 + t, ty = idx >> 4;
            const unsigned o = WS_Q + (unsigned)ty * (2u << 20) + (unsigned)((idx & 15) * 128 + tt) * 1024;
            const float sc = ty == 0 ? rs * (0.25f * 1.4426950408889634f) : rs;
            const u32x4 h0 = pack8(a, 0, sc), h1 = pack8(a, 8, sc);
            if (ty < 2) {
                __builtin_amdgcn_raw_buffer_store_b128(h0, rs_ws, lin, o, 16);
                __builtin_amdgcn_raw_buffer_store_b128(h1, rs_ws, lin, o + 65536, 16);
            } else {
                *(LAS u32x4*)tw = h0;
                const bf16x8 f0 = cat4(trd(tr), trd(tr + 256));
                *(LAS u32x4*)tw = h1;
                const bf16x8 f1 = cat4(trd(tr), trd(tr + 256));
                __builtin_amdgcn_raw_buffer_store_b128(__builtin_bit_cast(u32x4, f0), rs_ws, vo, o, 16);
                __builtin_amdgcn_raw_buffer_store_b128(__builtin_bit_cast(u32x4, f1), rs_ws, vo, o + 65536, 16);
            }
        }
    }
    STAMP(3);
    const int head = xcd * 4 + (i >> 3), qblk = i & 7;
    gcptr kvsrc = (gcptr)ws + (G ? WS_V : WS_K + 1024) + (size_t)head * 65536 + (((w >> 1) & 1) * 32 + (w & 1)) * 1024;
#define KV_POS(ch) ((((ch) >> 2) == 0 ? 8 : ((ch) >> 2) == 1 ? 0 : ((ch) >> 2) == 2 ? 4 : 12) + ((ch) & 3))
#define KV_LOAD(ch) glds16_dev(kvsrc + (ch) * 2048, lin, lds0 + KV_POS(ch) * 8192 + w * 1024)
    constexpr int NF = G ? 4 : 3;
    f32x16 ag[NF] = {};
    float cg[NF] = {};
    const lptr gl = smem + ((lane >> 4) & 1) * GU_UP + ((lane >> 5) * 8 + ((lane & 15) >> 2)) * 224 + (lane & 3) * 8 + G * 96;
    lptr gp = lbase(gl), gn = gp;
    u64 fl = 0;
    const int hp = head >> 1;
    const unsigned f1o = (unsigned)((((lane < 16 ? 16 + hp : lane < 32 ? 32 + hp : hp) / 3) * 16 + (lane & 15)) * 8);
#define G_OFF(m) ((((m) / NF) >> 1) * GU_SLOT + (((m) / NF) & 1) * 16 * 224 + ((m) % NF) * 32)
#define G_FRAG(P, m) cat4(trd(P + G_OFF(m)), trd(P + G_OFF(m) + 4 * 224))
#pragma unroll
    for (int m = 0; m < WD; ++m) wf[m] = G_FRAG(gp, m);
    SFOR(D, 8)
        SFOR(m, 4 * NF)
            constexpr int n = D * 4 * NF + m, ks = D * 4 + m / NF, f = m % NF;
            if (m == 4 * NF - WD && D < 7) {
                if (D == 0) { WAITV(0); BAR(); if (w == 0 && lane == 0) __hip_atomic_store((gu64*)(ws + WS_F1 + (j * 16 + rb) * 8), c.epoch, RLX_AGENT); }
                if (D == 1) { BAR(); if (G) GU_LOAD(5); }
                if (D == 2) { BAR(); if (!G) GU_LOAD(6); }
                if (D == 3) { BAR(); if (G) GU_LOAD(7); }
                if (D == 4) { if (G) WAITV(7); BAR(); if (!G) gload8_dev(fl, (gcptr)ws + WS_F1, f1o); }
                if (D == 5) {
                    if (!G) { WAITV(0); PIN(fl); if (w == 0 && !__all(fl == c.epoch)) poll_flags((gcptr)ws + WS_F1, f1o, c.epoch); }
                    BAR(); KV_LOAD(0); KV_LOAD(1); KV_LOAD(2); KV_LOAD(3);
                }
                if (D == 6) { if (G) WAITV(4); BAR(); KV_LOAD(4); KV_LOAD(5); KV_LOAD(6); }
                gn = lbase(gl + GU_PS(D + 1) * GU_PAIR);
                STAMP(23 + D);
            }
            ag[f] = MFMA32(wf[n % WD], PB(xr[ks]), ag[f]);
            PIN(ag[f]);
            if ((ks & 3) == tau) { cg[f] = sum8(wf[n % WD], cg[f]); PIN(cg[f]); }
            if (m + WD < 4 * NF) wf[n % WD] = G_FRAG(gp, m + WD);
            else if (D < 7) wf[n % WD] = G_FRAG(gn, m + WD - 4 * NF);
            SB();
        SEND
        gp = gn;
    SEND
#undef G_FRAG
#undef G_OFF
#undef GU_LOAD
#pragma unroll
    for (int f = 0; f < NF; ++f) *(LAS unsigned*)(smem + L_SCR + (G * 3 + f) * 1024 + lin + tau * 4) = split_hl(cg[f]);
    asm volatile("s_waitcnt vmcnt(3) lgkmcnt(0)\n\ts_barrier" ::: "memory");
    STAMP(4);
    {
        gcptr qp = (gcptr)ws + WS_Q + (size_t)(head * 64 + qblk * 8 + tau * 2) * 1024;
        gcptr kp = (gcptr)ws + WS_K + (size_t)head * 65536;
        gload16_dev(qa, qp, lin); gload16_dev(qb, qp + 1024, lin); gload16_dev(k0, kp, lin); gload16_dev(kc, kp + G * 32768, lin);
    }
#pragma unroll
    for (int ch = 7; ch < 16; ++ch) KV_LOAD(ch);
#undef KV_LOAD
    const float ns = rs * -1.4426950408889634f;
#pragma unroll
    for (int f = 0; f < NF; ++f) {
        const bf16x8 cf = *(LAS const bf16x8*)(smem + L_SCR + (G * 3 + f) * 1024 + lin);
        const f32x16 a = MFMA32(cf, muf, ag[f]);
        float hv[8];
#pragma unroll
        for (int e = 0; e < 8; ++e) {
            const float ex = __builtin_amdgcn_exp2f(a[e] * ns);
            hv[e] = a[8 + e] * __builtin_amdgcn_rcpf(__builtin_fmaf(ex, sd, sd));
        }
        const u32x4 pv = {pk2(hv[0], hv[1]), pk2(hv[2], hv[3]), pk2(hv[4], hv[5]), pk2(hv[6], hv[7])};
        __builtin_amdgcn_raw_buffer_store_b128(pv, rs_ws, lin, WS_H + (unsigned)(tt * 112 + j * 7 + G * 3 + f) * 1024, 16);
    }
    touch(w < 7 ? c.wd + (size_t)w * 262144 : c.wo, (unsigned)(i * 64 + lane) * 128, lds0 + L_DUMP);
    if (G) WAITV(14); else WAITV(13);
    PIN(qa); PIN(qb); PIN(k0); PIN(kc);
}

__device__ __forceinline__ void attn_slow(const __amdgpu_buffer_rsrc_t rs_ws, int head, bf16x8 q, int lane, f32x16& o) {
    float m = -__builtin_inff();
    o = f32x16{};
    const bool data = (lane & 16) == 0;
    const u32x4 ones = {0x3f803f80u, 0x3f803f80u, 0x3f803f80u, 0x3f803f80u};
    const unsigned vl = (unsigned)((lane >> 5) * 256 + (lane & 15) * 16);
#pragma nounroll
    for (int t = 0; t < 64; ++t) {
        const u32x4 k = __builtin_amdgcn_raw_buffer_load_b128(rs_ws, lane * 16, WS_K + head * 65536 + t * 1024, 16);
        u32x4 v0 = __builtin_amdgcn_raw_buffer_load_b128(rs_ws, vl, WS_V + head * 65536 + t * 1024, 16);
        u32x4 v1 = __builtin_amdgcn_raw_buffer_load_b128(rs_ws, vl + 512, WS_V + head * 65536 + t * 1024, 16);
        if (!data) { v0 = ones; v1 = ones; }
        f32x16 s = MFMA32(PB(k), q, f32x16{});
        const float mn = __builtin_fmaxf(m, half_swap_max(max16(s)));
        const float al = __builtin_amdgcn_exp2f(m - mn);
        m = mn;
#pragma unroll
        for (int r = 0; r < 16; ++r) { s[r] = __builtin_amdgcn_exp2f(s[r] - mn); o[r] *= al; }
        o = MFMA32(PB(v0), PB(pack8n(s, 0)), o);
        o = MFMA32(PB(v1), PB(pack8n(s, 8)), o);
    }
}

extern "C" __global__ __launch_bounds__(512, 2) void fused_block(const char* __restrict__ x_, const char* __restrict__ wq_, const char* __restrict__ wo_,
        const char* __restrict__ wg_, const char* __restrict__ wu_, const char* __restrict__ wd_, char* ws_, char* __restrict__ out_) {
    __shared__ __attribute__((aligned(16))) char smem_[163840];
    const int lane = threadIdx.x & 63;
    const int w = __builtin_amdgcn_readfirstlane(threadIdx.x >> 6);
    const int xcd = blockIdx.x & 7, i = blockIdx.x >> 3, G = w >> 2;
    const lptr smem = (lptr)smem_;
    const unsigned lds0 = (unsigned)(uintptr_t)smem, lin = lane * 16;
    gptr ws = (gptr)ws_;
    STAMP(0);
    if (w < 2) touch((gcptr)x_ + (size_t)(((xcd >> 1) * 4 + (i >> 3)) * 128 + (i & 7) * 16 + w * 8) * 1024, lane * 128, lds0 + L_DUMP);
    else if (w < 5) { const int L = (w - 2) * 2048 + i * 64 + lane; touch((gcptr)wq_ + (xcd & 1) * 1536, (unsigned)((L / 12) * 3072 + (L % 12) * 128), lds0 + L_DUMP); }
    else if (w == 5) {
        u64 pc; unsigned len;
        asm volatile("s_getpc_b64 %0\n.Lpc_ref:\n\ts_mov_b32 %1, .Lcode_end-.Lpc_ref-4" : "=s"(pc), "=s"(len));
        const unsigned o = (unsigned)(i * 64 + lane) * 128;
        touch((gcptr)pc, o < len ? o : len, lds0 + L_DUMP);
    }
    u64 ep;
    gload8_dev(ep, (gcptr)ws + WS_N, 0u);
    const __amdgpu_buffer_rsrc_t rs_ws = __builtin_amdgcn_make_buffer_rsrc((void*)ws_, 0, (int)WS_BYTES, 0x00020000);
    Ctx c;
    c.x = (gcptr)x_; c.wq = (gcptr)wq_; c.wo = (gcptr)wo_; c.wg = (gcptr)wg_; c.wu = (gcptr)wu_; c.wd = (gcptr)wd_; c.ws = ws;
    c.smem = smem; c.lds0 = lds0; c.lane = lane; c.w = w; c.xcd = xcd; c.i = i;
    u32x4 qa_, qb_, k0_, kc_;
    if (G) front<1>(c, ep, rs_ws, qa_, qb_, k0_, kc_); else front<0>(c, ep, rs_ws, qa_, qb_, k0_, kc_);
    const u64 epoch = c.epoch;
    STAMP(5);

    const int head = xcd * 4 + (i >> 3), qblk = i & 7;
    const int tb = xcd * 4 + (i >> 3), cb = i & 7;
    const int tt4 = tb * 2 + (w & 1), kq = w >> 1;
    const bf16x8 qa = PB(qa_), qb = PB(qb_);
    bf16x8 kc = PB(kc_);
    f32x16 ca, cb_, sa, sb;
    {
        const f32x16 s = MFMA32(PB(k0_), qa, f32x16{});
        const f32x16 s2 = MFMA32(PB(k0_), qb, f32x16{});
        const float na = -half_swap_max(max16(s)), nb = -half_swap_max(max16(s2));
#pragma unroll
        for (int r = 0; r < 16; ++r) { ca[r] = na; cb_[r] = nb; }
        sa = MFMA32(kc, qa, ca);
    }
    const int l15 = lane & 15;
    const bool data = (lane & 16) == 0;
    const unsigned vdat = 4096 + G * 2048 + (lane >> 5) * 256 + l15 * 16;
    const unsigned vone = L_ONES + (((l15 + 12) & 8) ? 0 : 64) + ((l15 & 11) ? 16 : 0);
    const unsigned vinc = data ? 8192u : 0u;
    f32x16 oa = {}, ob = {};
    u32x4 p0 = {0u, 0u, 0u, 0u}, p1 = {0u, 0u, 0u, 0u};
    bf16x8 v0, v1 = {0, 0, 0, 0, 0, 0, 0, 0};
    const unsigned bvo = (unsigned)((lane >> 3) * 1024 + ((((lane >> 2) ^ (lane >> 4)) & 1) * 64) + (lane & 3) * 16);
    gcptr bd = (gcptr)wd_ + w * 8192 + cb * 128, bo = (gcptr)wo_ + w * 8192 + cb * 128;
#define B_POS(n) ((n) % 19 < 4 ? 8 + (n) % 19 : (n) % 19 < 12 ? (n) % 19 - 4 : (n) % 19)
#define B_LOAD(n) glds16((n) < 28 ? bd + (n) * 65536 : bo + ((((n) - 28) + cb) & 7) * 65536, bvo, lds0 + B_POS(n) * 8192 + w * 1024)
#define EXPR(S, a_, b_) do { _Pragma("unroll") for (int r = (a_); r < (b_); ++r) S[r] = __builtin_amdgcn_exp2f(S[r]); } while (0)
#define HALF(SC, MF_A, MF_B, MF_C, LD_A, LD_B) do { \
        MF_A; SB(); LD_A; EXPR(SC, 0, 5); PIN(SC); SB(); \
        MF_B; SB(); LD_B; EXPR(SC, 5, 8); p0 = pack8n(SC, 0); PIN(p0); SB(); EXPR(SC, 8, 10); PIN(SC); SB(); \
        MF_C; SB(); EXPR(SC, 10, 16); PIN(SC); p1 = pack8n(SC, 8); PIN(p1); SB(); \
    } while (0)
#define TILE(ko, vo) do { \
        HALF(sa, do { sb = MFMA32(kc, qb, cb_); PIN(sb); } while (0), do { ob = MFMA32(v1, PB(p1), ob); PIN(ob); } while (0), do { oa = MFMA32(v0, PB(p0), oa); PIN(oa); } while (0), \
             do { kc = *(LAS const bf16x8*)(kp + (ko)); v0 = *(LAS const bf16x8*)(vp + (vo)); } while (0), v1 = *(LAS const bf16x8*)(vp + (vo) + 512)); \
        HALF(sb, do { sa = MFMA32(kc, qa, ca); PIN(sa); } while (0), do { oa = MFMA32(v1, PB(p1), oa); PIN(oa); } while (0), do { ob = MFMA32(v0, PB(p0), ob); PIN(ob); } while (0), (void)0, (void)0); \
    } while (0)
#define CHUNK(q) do { __builtin_amdgcn_s_setprio(3 - (q)); TILE((q) * 8192, 0); TILE((q) * 8192 + 1024, 1024); vp = lbase(vp + vinc); } while (0)
#define SUPER(pos) do { \
        const lptr kp = lbase(smem + (pos) * 8192 + G * 2048 + lin); \
        lptr vp = lbase(smem + (data ? vdat + (pos) * 8192 : vone)); \
        CHUNK(0); CHUNK(1); CHUNK(2); CHUNK(3); \
    } while (0)
    u64 fl = 0;
    const unsigned f2o = (unsigned)(((tb >> 1) * 16 + l15) * 8);
#pragma nounroll
    for (int sc = 0; sc < 2; ++sc) {
        SUPER(8 - sc * 8);
        if (sc == 0) {
            asm volatile("s_waitcnt vmcnt(0) lgkmcnt(0)\n\ts_barrier" ::: "memory");
            if (w == 0 && lane == 0) __hip_atomic_store((gu64*)(ws + WS_F2 + (((xcd >> 1) * 4 + (i >> 3)) * 16 + (xcd & 1) * 8 + (i & 7)) * 8), epoch, RLX_AGENT);
            B_LOAD(0); B_LOAD(1); B_LOAD(2); B_LOAD(3);
            if (w == 7) touch((gcptr)wo_ + 262144, (unsigned)(i * 64 + lane) * 128, lds0 + L_DUMP);
            if (w == 1 && blockIdx.x == 0) {
#pragma unroll
                for (int q = 0; q < 4; ++q) poll_flags((gcptr)ws + WS_F1, (unsigned)(q * 64 + lane) * 8, epoch);
                if (lane == 0) __hip_atomic_store((gu64*)(ws + WS_N), epoch, RLX_AGENT);
            }
        }
    }
    if (w == 0) poll_flags((gcptr)ws + WS_F2, f2o, epoch);
    asm volatile("s_waitcnt lgkmcnt(0)\n\ts_barrier" ::: "memory");
    B_LOAD(4); B_LOAD(5); B_LOAD(6); B_LOAD(7);
    constexpr int HP = 24;
    u32x4 hf[28];
    gcptr hsrc = (gcptr)ws + WS_H + (size_t)(tt4 * 112 + kq) * 1024;
#pragma unroll
    for (int n = 0; n < HP; ++n) gload16_dev(hf[n], hsrc + n * 4096, lin);
#pragma nounroll
    for (int sc = 2; sc < 4; ++sc) {
        SUPER(sc * 8 - 12);
        if (sc == 2) {
            asm volatile("s_waitcnt lgkmcnt(0)\n\ts_barrier" ::: "memory");
            B_LOAD(8); B_LOAD(9); B_LOAD(10); B_LOAD(11);
        }
    }
    ob = MFMA32(v1, PB(p1), ob);
#undef SUPER
#undef CHUNK
#undef TILE
#undef HALF
#undef EXPR
    STAMP(6);
    {
        const f32x16 snd = G ? oa : ob;
        const lptr ex = smem + L_COMB + w * 3072 + lin;
        *(LAS f32x4*)(ex) = f32x4{snd[0], snd[1], snd[2], snd[3]};
        *(LAS f32x4*)(ex + 1024) = f32x4{snd[4], snd[5], snd[6], snd[7]};
        *(LAS float*)(ex + 2048) = snd[8];
    }
    asm volatile("s_waitcnt vmcnt(0) lgkmcnt(0)\n\ts_barrier" ::: "memory");
    {
        f32x16 o = G ? ob : oa;
        const lptr ex = smem + L_COMB + (w ^ 4) * 3072 + lin;
        const f32x4 e0 = *(LAS const f32x4*)(ex), e1 = *(LAS const f32x4*)(ex + 1024);
        const float e2 = *(LAS const float*)(ex + 2048);
#pragma unroll
        for (int r = 0; r < 4; ++r) { o[r] += e0[r]; o[4 + r] += e1[r]; }
        o[8] += e2;
        bool ok = o[8] <= 1.0995116e12f;
#pragma unroll
        for (int r = 0; r < 8; ++r) ok = ok && (__builtin_fabsf(o[r]) <= 3.0e38f);
        if (__builtin_expect(!__all(ok), 0)) attn_slow(rs_ws, head, G ? qb : qa, lane, o);
        const float rl = 1.f / o[8];
        __builtin_amdgcn_raw_buffer_store_b128(pack8(o, 0, rl), rs_ws, lin, WS_O + (unsigned)((qblk * 8 + (w & 3) * 2 + G) * 32 + head) * 1024, 16);
    }
    STAMP(7);
#pragma unroll
    for (int n = HP; n < 28; ++n) gload16_dev(hf[n], hsrc + n * 4096, lin);
    u32x4 xv;
    const unsigned xo = (unsigned)((tb * 64 + w * 8 + (lane >> 3)) * 1024 + cb * 128 + (lane & 7) * 16);
    gload16(xv, (gcptr)x_, xo);
    B_LOAD(12); B_LOAD(13); B_LOAD(14); B_LOAD(15);
#pragma unroll
    for (int n = 0; n < HP; ++n) PIN(hf[n]);
    const int dq = l15 >> 2;
    lptr db[2][3];
#pragma unroll
    for (int t = 0; t < 2; ++t) {
        db[t][0] = lbase(smem + (kq * 16 + (lane >> 5) * 4 + dq) * 128 + (((dq >> 1) & 1) ^ t) * 64 + ((lane >> 4) & 1) * 32 + (lane & 3) * 8);
        db[t][1] = lbase(db[t][0] + 65536); db[t][2] = lbase(db[t][0] + 131072);
    }
    constexpr int WD = 4;
    bf16x8 wf[WD];
    f32x16 dd[2] = {};
#define D_FRAG(n) cat4(trd(db[(n) & 1][B_POS((n) >> 1) >> 3] + (B_POS((n) >> 1) & 7) * 8192), trd(db[(n) & 1][B_POS((n) >> 1) >> 3] + (B_POS((n) >> 1) & 7) * 8192 + 1024))
#pragma unroll
    for (int m = 0; m < WD; ++m) wf[m] = D_FRAG(m);
#define D_STEP(n) do { dd[(n) & 1] = MFMA32(wf[(n) % WD], PB(hf[(n) >> 1]), dd[(n) & 1]); PIN(dd[(n) & 1]); \
        if ((n) + WD < 56) wf[(n) % WD] = D_FRAG(((n) + WD) % 56); SB(); } while (0)
#define D_STEP4(n) do { D_STEP(n); D_STEP((n) + 1); D_STEP((n) + 2); D_STEP((n) + 3); } while (0)
    D_STEP4(0);
    WAITV(9); BAR();
    if (w == 0 && lane == 0) __hip_atomic_store((gu64*)(ws + WS_F3 + (qblk * 32 + head) * 8), epoch, RLX_AGENT);
    B_LOAD(16); B_LOAD(17); B_LOAD(18);
    D_STEP4(4); D_STEP4(8);
    BAR(); B_LOAD(19); B_LOAD(20); B_LOAD(21); B_LOAD(22);
    D_STEP4(12); D_STEP4(16);
    WAITV(7); BAR(); B_LOAD(23); B_LOAD(24); B_LOAD(25); B_LOAD(26);
    PIN(hf[24]); PIN(hf[25]); PIN(hf[26]); PIN(hf[27]); PIN(xv);
    D_STEP4(20); D_STEP4(24);
    WAITV(7); BAR(); B_LOAD(27); B_LOAD(28); B_LOAD(29); B_LOAD(30);
    D_STEP4(28); D_STEP4(32);
    WAITV(7); BAR(); B_LOAD(31); B_LOAD(32); B_LOAD(33); B_LOAD(34);
    D_STEP4(36); D_STEP4(40);
    WAITV(7); BAR(); B_LOAD(35);
    D_STEP4(44); D_STEP4(48); D_STEP4(52);
#undef D_STEP4
#undef D_STEP
#undef D_FRAG
    STAMP(8);
    const int prow = (w & 1) * 32 + (lane & 31);
    const lptr pw = smem + (kq & 1) * 16384 + (kq >> 1) * 65536 + prow * 256;
    const int rrow = w * 8 + (lane >> 3);
    const lptr pr0 = smem + rrow * 256 + (((lane & 7) * 2) ^ (rrow & 7)) * 16, pr1 = smem + rrow * 256 + (((lane & 7) * 2 + 1) ^ (rrow & 7)) * 16;
#define PART_WRITE(A) do { _Pragma("unroll") for (int t = 0; t < 2; ++t) _Pragma("unroll") for (int g = 0; g < 4; ++g) \
        *(LAS f32x4*)(pw + ((t * 8 + g * 2 + (lane >> 5)) ^ (prow & 7)) * 16) = f32x4{A[t][4 * g], A[t][4 * g + 1], A[t][4 * g + 2], A[t][4 * g + 3]}; } while (0)
#define PART_READ(lo, hi) do { \
        lo = (*(LAS const f32x4*)(pr0) + *(LAS const f32x4*)(pr0 + 16384)) + (*(LAS const f32x4*)(pr2) + *(LAS const f32x4*)(pr2 + 16384)); \
        hi = (*(LAS const f32x4*)(pr1) + *(LAS const f32x4*)(pr1 + 16384)) + (*(LAS const f32x4*)(pr3) + *(LAS const f32x4*)(pr3 + 16384)); } while (0)
    const lptr pr2 = lbase(pr0 + 65536), pr3 = lbase(pr1 + 65536);
    BAR();
    PART_WRITE(dd);
    asm volatile("s_waitcnt vmcnt(0) lgkmcnt(0)\n\ts_barrier" ::: "memory");
    f32x4 glo, ghi;
    PART_READ(glo, ghi);
#pragma unroll
    for (int e = 0; e < 4; ++e) {
        glo[e] = glo[e] * __builtin_amdgcn_rcpf(1.f + __builtin_amdgcn_exp2f(glo[e] * -1.4426950408889634f));
        ghi[e] = ghi[e] * __builtin_amdgcn_rcpf(1.f + __builtin_amdgcn_exp2f(ghi[e] * -1.4426950408889634f));
    }
    PIN(glo); PIN(ghi);
    BAR();
#pragma unroll
    for (int t = 0; t < 2; ++t) {
        db[t][0] = lbase(smem + (kq * 16 + (lane >> 5) * 8 + dq) * 128 + (((dq >> 1) & 1) ^ t) * 64 + ((lane >> 4) & 1) * 32 + (lane & 3) * 8);
        db[t][1] = lbase(db[t][0] + 65536); db[t][2] = lbase(db[t][0] + 131072);
    }
    bf16x8 wo_f[16];
#pragma unroll
    for (int n = 0; n < 16; ++n) {
        const int ps = B_POS(28 + (n >> 1));
        wo_f[n] = cat4(trd(db[n & 1][ps >> 3] + (ps & 7) * 8192), trd(db[n & 1][ps >> 3] + (ps & 7) * 8192 + 512));
        PIN(wo_f[n]);
    }
    STAMP(9);
    poll_flags((gcptr)ws + WS_F3, (unsigned)(((tb >> 2) * 32 + (lane & 31)) * 8), epoch);
    STAMP(10);
    u32x4 of[8];
    {
        gcptr op = (gcptr)ws + WS_O + (size_t)(tt4 * 32 + kq) * 1024;
#pragma unroll
        for (int m = 0; m < 8; ++m) gload16_dev(of[m], op + ((m + cb) & 7) * 4096, lin);
    }
    f32x16 ss[2] = {};
#define O_STEP(m, N) do { WAITV(N); PIN(of[m]); ss[0] = MFMA32(wo_f[2 * (m)], PB(of[m]), ss[0]); ss[1] = MFMA32(wo_f[2 * (m) + 1], PB(of[m]), ss[1]); PIN(ss[0]); PIN(ss[1]); } while (0)
    O_STEP(0, 7); O_STEP(1, 6); O_STEP(2, 5); O_STEP(3, 4); O_STEP(4, 3); O_STEP(5, 2); O_STEP(6, 1); O_STEP(7, 0);
#undef O_STEP
    STAMP(11);
    PART_WRITE(ss);
    asm volatile("s_waitcnt lgkmcnt(0)\n\ts_barrier" ::: "memory");
    f32x4 slo, shi;
    PART_READ(slo, shi);
    const u32x4 ov = {pk2(__builtin_fmaf(slo[0], glo[0], bf_lo(xv[0])), __builtin_fmaf(slo[1], glo[1], bf_hi(xv[0]))),
                      pk2(__builtin_fmaf(slo[2], glo[2], bf_lo(xv[1])), __builtin_fmaf(slo[3], glo[3], bf_hi(xv[1]))),
                      pk2(__builtin_fmaf(shi[0], ghi[0], bf_lo(xv[2])), __builtin_fmaf(shi[1], ghi[1], bf_hi(xv[2]))),
                      pk2(__builtin_fmaf(shi[2], ghi[2], bf_lo(xv[3])), __builtin_fmaf(shi[3], ghi[3], bf_hi(xv[3])))};
    *(GAS u32x4*)((gptr)out_ + xo) = ov;
    STAMP(12);
    asm volatile(".Lcode_end:");
}

extern "C" void kernel_launch(void* const* d_in, const int* in_sizes, int n_in,
                              void* d_out, int out_size, void* d_ws, size_t ws_size,
                              hipStream_t stream) {
    fused_block<<<256, 512, 0, stream>>>((const char*)d_in[0], (const char*)d_in[1], (const char*)d_in[2], (const char*)d_in[4],
                                         (const char*)d_in[5], (const char*)d_in[6], (char*)d_ws, (char*)d_out);
}
```

```cpp
#include <hip/hip_runtime.h>
#include <hip/hip_bf16.h>
#include <stdint.h>

typedef short bf16x8 __attribute__((ext_vector_type(8)));
typedef short s16x4 __attribute__((ext_vector_type(4)));
typedef float f32x16 __attribute__((ext_vector_type(16)));
typedef float f32x4 __attribute__((ext_vector_type(4)));
typedef float f32x2 __attribute__((ext_vector_type(2)));
typedef __bf16 bfx2 __attribute__((ext_vector_type(2)));
typedef unsigned u32x4 __attribute__((ext_vector_type(4)));
typedef unsigned u32x2 __attribute__((ext_vector_type(2)));
typedef unsigned long long u64;
#define LAS __attribute__((address_space(3)))
#define GAS __attribute__((address_space(1)))
typedef LAS char* lptr;
typedef GAS const char* gcptr;
typedef GAS char* gptr;
typedef GAS u64 gu64;

#define MFMA32(a, b, c) __builtin_amdgcn_mfma_f32_32x32x16_bf16((a), (b), (c), 0, 0, 0)
#define MFMA16(a, b, c) __builtin_amdgcn_mfma_f32_16x16x32_bf16((a), (b), (c), 0, 0, 0)
#define RLX_AGENT __ATOMIC_RELAXED, __HIP_MEMORY_SCOPE_AGENT
#define RLX_WG __ATOMIC_RELAXED, __HIP_MEMORY_SCOPE_WORKGROUP
#define PB(x) __builtin_bit_cast(bf16x8, x)

constexpr unsigned WS_Q = 2u << 20;
constexpr unsigned WS_K = 4u << 20;
constexpr unsigned WS_V = 6u << 20;
constexpr unsigned WS_O = 8u << 20;
constexpr unsigned WS_H = 14u << 20;
constexpr unsigned WS_N = 22u << 20;
constexpr unsigned WS_F1 = WS_N + 4096;
constexpr unsigned WS_F2 = WS_N + 8192;
constexpr unsigned WS_F3 = WS_N + 12288;
constexpr unsigned WS_BYTES = 24u << 20;

constexpr int L_COMB = 131072;
constexpr int L_MISC = 155648;
constexpr int L_ONES = L_MISC;
constexpr int L_DUMP = L_MISC + 2048;
constexpr int L_CS = L_MISC + 2304;
constexpr int L_STAT = L_MISC + 4608;

#define STAMP(n) do {} while (0)
#define SB() __builtin_amdgcn_sched_barrier(0)
#define PIN(x) asm volatile("" : "+v"(x))
#define BAR() asm volatile("s_barrier" ::: "memory")
#define WAITV(n) asm volatile("s_waitcnt vmcnt(" #n ")" ::: "memory")
#define WAITL() asm volatile("s_waitcnt lgkmcnt(0)" ::: "memory")
__device__ __forceinline__ unsigned pk2(float lo, float hi) {
    f32x2 v = {lo, hi};
    return __builtin_bit_cast(unsigned, __builtin_convertvector(v, bfx2));
}
__device__ __forceinline__ float bf_lo(unsigned u) { return __uint_as_float(u << 16); }
__device__ __forceinline__ float bf_hi(unsigned u) { return __uint_as_float(u & 0xffff0000u); }
__device__ __forceinline__ float rbf(float f) { return bf_lo(pk2(f, f) & 0xffffu); }
__device__ __forceinline__ void glds16(gcptr sbase, unsigned voff, unsigned lds_base) {
    asm volatile("s_mov_b32 m0, %2\n\ts_nop 0\n\tglobal_load_lds_dwordx4 %0, %1" :: "v"(voff), "s"(sbase), "s"(lds_base) : "memory");
}
__device__ __forceinline__ void glds16_dev(gcptr sbase, unsigned voff, unsigned lds_base) {
    asm volatile("s_mov_b32 m0, %2\n\ts_nop 0\n\tglobal_load_lds_dwordx4 %0, %1 sc1" :: "v"(voff), "s"(sbase), "s"(lds_base) : "memory");
}
__device__ __forceinline__ void touch(gcptr sbase, unsigned voff, unsigned lds_dump) {
    asm volatile("s_mov_b32 m0, %2\n\ts_nop 0\n\tglobal_load_lds_dword %0, %1" :: "v"(voff), "s"(sbase), "s"(lds_dump) : "memory");
}
__device__ __forceinline__ void gload16_dev(u32x4& d, gcptr sbase, unsigned voff) {
    asm volatile("global_load_dwordx4 %0, %1, %2 sc1" : "=v"(d) : "v"(voff), "s"(sbase) : "memory");
}
__device__ __forceinline__ void gload16(u32x4& d, gcptr sbase, unsigned voff) {
    asm volatile("global_load_dwordx4 %0, %1, %2" : "=v"(d) : "v"(voff), "s"(sbase) : "memory");
}
__device__ __forceinline__ void gload8_dev(u64& d, gcptr sbase, unsigned voff) {
    asm volatile("global_load_dwordx2 %0, %1, %2 sc1" : "=v"(d) : "v"(voff), "s"(sbase) : "memory");
}
__device__ __forceinline__ s16x4 trd(lptr p) { return __builtin_amdgcn_ds_read_tr16_b64_v4i16((LAS s16x4*)p); }
__device__ __forceinline__ bf16x8 cat4(s16x4 a, s16x4 b) { return __builtin_shufflevector(a, b, 0, 1, 2, 3, 4, 5, 6, 7); }
__device__ __forceinline__ u32x4 pack8(const f32x16& a, int o, float s) {
    u32x4 r;
    r[0] = pk2(a[o + 0] * s, a[o + 1] * s); r[1] = pk2(a[o + 2] * s, a[o + 3] * s);
    r[2] = pk2(a[o + 4] * s, a[o + 5] * s); r[3] = pk2(a[o + 6] * s, a[o + 7] * s);
    return r;
}
__device__ __forceinline__ u32x4 pack8n(const f32x16& a, int o) {
    u32x4 r;
    r[0] = pk2(a[o + 0], a[o + 1]); r[1] = pk2(a[o + 2], a[o + 3]);
    r[2] = pk2(a[o + 4], a[o + 5]); r[3] = pk2(a[o + 6], a[o + 7]);
    return r;
}
__device__ __forceinline__ float half_swap_add(float v) {
    auto r = __builtin_amdgcn_permlane32_swap(__float_as_uint(v), __float_as_uint(v), false, false);
    return __uint_as_float(r[0]) + __uint_as_float(r[1]);
}
__device__ __forceinline__ float half_swap_max(float v) {
    auto r = __builtin_amdgcn_permlane32_swap(__float_as_uint(v), __float_as_uint(v), false, false);
    return __builtin_fmaxf(__uint_as_float(r[0]), __uint_as_float(r[1]));
}
__device__ __forceinline__ float max16(const f32x16& s) {
    float a = __builtin_fmaxf(__builtin_fmaxf(s[0], s[1]), s[2]), b = __builtin_fmaxf(__builtin_fmaxf(s[3], s[4]), s[5]);
    a = __builtin_fmaxf(__builtin_fmaxf(a, s[6]), s[7]);   b = __builtin_fmaxf(__builtin_fmaxf(b, s[8]), s[9]);
    a = __builtin_fmaxf(__builtin_fmaxf(a, s[10]), s[11]); b = __builtin_fmaxf(__builtin_fmaxf(b, s[12]), s[13]);
    a = __builtin_fmaxf(__builtin_fmaxf(a, s[14]), s[15]);
    return __builtin_fmaxf(a, b);
}
__device__ __forceinline__ lptr lbase(lptr p) { unsigned u = (unsigned)(uintptr_t)p; asm volatile("" : "+v"(u)); return (lptr)(uintptr_t)u; }
__device__ __forceinline__ float dot2(unsigned a, unsigned b, float c) {
    return __builtin_amdgcn_fdot2_f32_bf16(__builtin_bit_cast(bfx2, a), __builtin_bit_cast(bfx2, b), c, false);
}
__device__ __forceinline__ unsigned split_hl(float v) {
    const float hi = rbf(v);
    return pk2(hi, v - hi);
}
__device__ __forceinline__ void poll_flags(gcptr base, unsigned voff, u64 epoch) {
#pragma nounroll
    for (unsigned spins = 0; spins < (1u << 20); ++spins) {
        const u64 v = __hip_atomic_load((GAS const u64*)(base + voff), RLX_AGENT);
        if (__all(v == epoch)) break;
        __builtin_amdgcn_s_sleep(2);
    }
    asm volatile("" ::: "memory");
}

template <int V> struct IC { static constexpr int value = V; };
template <int I, int N, class F> __device__ __forceinline__ void static_for(F&& f) {
    if constexpr (I < N) { f(IC<I>{}); static_for<I + 1, N>(f); }
}
#define SFOR(var, N) static_for<0, (N)>([&](auto ic_##var) __attribute__((always_inline)) { constexpr int var = decltype(ic_##var)::value;
#define SEND });

__device__ __forceinline__ void attn_slow(const __amdgpu_buffer_rsrc_t rs_ws, int head, bf16x8 q, int lane, f32x16& o) {
    float m = -__builtin_inff();
    o = f32x16{};
    const bool data = (lane & 16) == 0;
    const u32x4 ones = {0x3f803f80u, 0x3f803f80u, 0x3f803f80u, 0x3f803f80u};
    const unsigned vl = (unsigned)((lane >> 5) * 256 + (lane & 15) * 16);
#pragma nounroll
    for (int t = 0; t < 64; ++t) {
        const u32x4 k = __builtin_amdgcn_raw_buffer_load_b128(rs_ws, lane * 16, WS_K + head * 65536 + t * 1024, 16);
        u32x4 v0 = __builtin_amdgcn_raw_buffer_load_b128(rs_ws, vl, WS_V + head * 65536 + t * 1024, 16);
        u32x4 v1 = __builtin_amdgcn_raw_buffer_load_b128(rs_ws, vl + 512, WS_V + head * 65536 + t * 1024, 16);
        if (!data) { v0 = ones; v1 = ones; }
        f32x16 s = MFMA32(PB(k), q, f32x16{});
        const float mn = __builtin_fmaxf(m, half_swap_max(max16(s)));
        const float al = __builtin_amdgcn_exp2f(m - mn);
        m = mn;
#pragma unroll
        for (int r = 0; r < 16; ++r) { s[r] = __builtin_amdgcn_exp2f(s[r] - mn); o[r] *= al; }
        o = MFMA32(PB(v0), PB(pack8n(s, 0)), o);
        o = MFMA32(PB(v1), PB(pack8n(s, 8)), o);
    }
}

extern "C" __global__ __launch_bounds__(512, 2) void fused_block(const char* __restrict__ x_, const char* __restrict__ wq_, const char* __restrict__ wo_,
        const char* __restrict__ wg_, const char* __restrict__ wu_, const char* __restrict__ wd_, char* ws_, char* __restrict__ out_) {
    __shared__ __attribute__((aligned(16))) char smem_[163840];
    const int lane = threadIdx.x & 63;
    const int w = __builtin_amdgcn_readfirstlane(threadIdx.x >> 6);
    const int xcd = blockIdx.x & 7, i = blockIdx.x >> 3, G = w >> 2, tau = w & 3;
    const lptr smem = (lptr)smem_;
    const unsigned lds0 = (unsigned)(uintptr_t)smem, lin = lane * 16;
    gptr ws = (gptr)ws_;
    if (w == 7) {
        u64 pc; unsigned len;
        asm volatile("s_getpc_b64 %0\n.Lpc_ref:\n\ts_mov_b32 %1, .Lcode_end-.Lpc_ref-4" : "=s"(pc), "=s"(len));
        const unsigned o = (unsigned)(i * 64 + lane) * 128;
        touch((gcptr)pc, o < len ? o : len, lds0 + L_DUMP);
    }
    STAMP(0);
    const int rbl = i >> 2, jl = i & 3, cq = xcd & 3;
    const int rb = (xcd >> 2) * 8 + rbl, tt = rb * 4 + tau, rot = (rbl + 2 * jl) & 7;
    const bool tyb = jl == 3;
    {
        const unsigned v0 = (unsigned)((lane >> 3) * 1024 + (((lane & 7) ^ (lane >> 4)) * 16));
        const unsigned v1 = (unsigned)((8 + (lane >> 3)) * 1024 + (((lane & 7) ^ (4 + (lane >> 4))) * 16));
        gcptr xp = (gcptr)x_ + (size_t)(tt * 32 + G * 16) * 1024;
        const unsigned xd = lds0 + tau * 4096 + G * 2048;
#pragma nounroll
        for (int u = 0; u < 8; ++u) {
            gcptr xs = xp + ((u + rot) & 7) * 128;
            glds16(xs, v0, xd + (u ^ 4) * 16384); glds16(xs, v1, xd + (u ^ 4) * 16384 + 1024);
        }
    }
    const unsigned sw0 = (unsigned)(((((lane >> 2) ^ (lane >> 4)) & 1) * 64) + (lane & 3) * 16), sw1 = sw0 ^ 32u;
    const unsigned vq = (lane >> 3) * 3072 + sw0;
    gcptr s1p = (gcptr)wq_ + (6 * cq + jl) * 128 + w * 8 * 3072;
#define S1_POS(D) ((D) < 3 ? 16 + (D) : 8 + (D))
#define S1_LOAD(D) glds16(s1p + (size_t)(((D) + rot) & 7) * 64 * 3072, vq, lds0 + S1_POS(D) * 8192 + w * 1024)
    S1_LOAD(0); S1_LOAD(1); S1_LOAD(2);
    u64 ep;
    gload8_dev(ep, (gcptr)ws + WS_N, 0u);
    const __amdgpu_buffer_rsrc_t rs_ws = __builtin_amdgcn_make_buffer_rsrc((void*)ws_, 0, (int)WS_BYTES, 0x00020000);
    if (w < 2) {
        const unsigned o = (lane & 1) ? 0u : 0x3f803f80u;
        *(LAS u32x4*)(smem + L_ONES + w * 1024 + lin) = u32x4{o, o, o, o};
    }
    const int p2 = tyb ? 3072 : 3584;
    const unsigned v2a = (lane >> 3) * p2 + sw0, v2b = (lane >> 3) * p2 + sw1, v3a = (lane >> 3) * 3584 + sw0, v3b = (lane >> 3) * 3584 + sw1;
    const int fu2 = 7 * cq + 2 * jl, fu3 = tyb ? fu2 : fu2 + 1;
    gcptr g2a = (tyb ? (gcptr)wq_ + (6 * cq + 4) * 128 : (gcptr)wg_ + fu2 * 128) + w * 8 * p2;
    gcptr g2b = (tyb ? (gcptr)wq_ + (6 * cq + 5) * 128 : (gcptr)wu_ + fu2 * 128) + w * 8 * p2;
    gcptr s3a = (gcptr)wg_ + fu3 * 128 + w * 8 * 3584, s3b = (gcptr)wu_ + fu3 * 128 + w * 8 * 3584;
#define S2_LOAD(D) do { const size_t o_ = (size_t)(((D) + rot) & 7) * 64 * p2; \
        glds16(g2a + o_, v2a, lds0 + (D) * 16384 + w * 1024); glds16(g2b + o_, v2b, lds0 + (D) * 16384 + 8192 + w * 1024); } while (0)
#define S3_LOAD(D) do { const size_t o_ = (size_t)(((D) + rot) & 7) * 64 * 3584; \
        glds16(s3a + o_, v3a, lds0 + (D) * 16384 + w * 1024); glds16(s3b + o_, v3b, lds0 + (D) * 16384 + 8192 + w * 1024); } while (0)
    const int l15 = lane & 15, dq = l15 >> 2, kr = tau + 2 * G;
    u32x4 xr[32];
    lptr xb[4], wb[4][2];
#pragma unroll
    for (int q = 0; q < 4; ++q) {
        xb[q] = lbase(smem + 65536 + tau * 4096 + (lane & 31) * 128 + (((2 * ((q + kr) & 3) + (lane >> 5)) ^ ((lane >> 1) & 7)) * 16));
        wb[q][0] = lbase(smem + 90112 + (((q + kr) & 3) * 16 + (lane >> 5) * 8 + dq) * 128 + ((G ^ (dq >> 1)) & 1) * 64 + ((lane >> 4) & 1) * 32 + (lane & 3) * 8);
    }
    unsigned onev = 0x3f803f80u;
    PIN(onev);
    const unsigned selv = l15 == ((lane >> 4) & 1) ? 0x3f803f80u : 0u;
    const bf16x8 self = PB((u32x4{selv, selv, selv, selv}));
    float s1 = 0.f, s2 = 0.f, s1b = 0.f, s2b = 0.f;
#define X_STAT(n) do { s1 = dot2(xr[n][0], onev, s1); s2 = dot2(xr[n][0], xr[n][0], s2); s1b = dot2(xr[n][1], onev, s1b); s2b = dot2(xr[n][1], xr[n][1], s2b); \
        s1 = dot2(xr[n][2], onev, s1); s2 = dot2(xr[n][2], xr[n][2], s2); s1b = dot2(xr[n][3], onev, s1b); s2b = dot2(xr[n][3], xr[n][3], s2b); } while (0)
    WAITV(12); BAR();
    STAMP(1);
#pragma unroll
    for (int n = 0; n < 16; ++n) xr[n] = *(LAS const u32x4*)(xb[n & 3] + (n >> 2) * 16384);
    WAITL(); BAR();
    S1_LOAD(3); S1_LOAD(4); S1_LOAD(5); S1_LOAD(6); S1_LOAD(7);
#pragma unroll
    for (int u = 0; u < 4; ++u) { X_STAT(4 * u); X_STAT(4 * u + 1); }
    PIN(s1); PIN(s2); PIN(s1b); PIN(s2b);
#pragma unroll
    for (int q = 0; q < 4; ++q) xb[q] = lbase(xb[q] - 65536);
    WAITV(9); BAR();
    STAMP(2);
#pragma unroll
    for (int n = 16; n < 32; ++n) xr[n] = *(LAS const u32x4*)(xb[n & 3] + ((n >> 2) - 4) * 16384);
    asm volatile("s_waitcnt vmcnt(5) lgkmcnt(0)\n\ts_barrier" ::: "memory");
    S2_LOAD(0); S2_LOAD(1); S2_LOAD(2); S2_LOAD(3); S2_LOAD(4);
    PIN(ep);
    const u64 epoch = (((u64)(unsigned)__builtin_amdgcn_readfirstlane((unsigned)(ep >> 32)) << 32) | (unsigned)__builtin_amdgcn_readfirstlane((unsigned)ep)) + 1ull;
    constexpr int WD = 4;
    bf16x8 wf[WD];
#define S1_FRAG(n) cat4(trd(wb[(n) & 3][0] + (S1_POS((n) >> 2) - 11) * 8192), trd(wb[(n) & 3][0] + (S1_POS((n) >> 2) - 11) * 8192 + 512))
#pragma unroll
    for (int m = 0; m < WD; ++m) wf[m] = S1_FRAG(m);
#pragma unroll
    for (int u = 4; u < 8; ++u) { X_STAT(4 * u); X_STAT(4 * u + 1); }
#undef X_STAT
    *(LAS f32x2*)(smem + L_STAT + w * 256 + (lane & 31) * 8) = f32x2{half_swap_add(s1 + s1b), half_swap_add(s2 + s2b)};
    STAMP(3);
    f32x16 ac[2];
    f32x4 cs[2];
    const lptr csw = smem + L_CS + G * 1024 + ((lane & 1) * 16 + (lane >> 4) * 4) * 16 + tau * 4;
    const lptr csr = smem + (lane < 32 ? L_CS + G * 1024 + lin : L_ONES + 16);
    const unsigned csi = lane < 32 ? 512u : 0u;
#define CS_WRITE(m) do { if (l15 < 2) { _Pragma("unroll") for (int r = 0; r < 4; ++r) *(LAS unsigned*)(csw + (m) * 512 + r * 16) = split_hl(cs[m][r]); } } while (0)
    ac[0] = f32x16{}; cs[0] = f32x4{};
    SFOR(n, 32)
        if constexpr (n == 12 - WD) { WAITV(10); BAR(); }
        ac[0] = MFMA32(wf[n % WD], PB(xr[n]), ac[0]);
        PIN(ac[0]);
        if constexpr ((n & 3) == 0) { cs[0] = MFMA16(wf[n % WD], self, cs[0]); PIN(cs[0]); }
        if constexpr (n + WD < 32) wf[n % WD] = S1_FRAG(n + WD);
        SB();
    SEND
#undef S1_FRAG
    CS_WRITE(0);
#pragma unroll
    for (int q = 0; q < 4; ++q) {
        const lptr b0 = smem + ((lane >> 4) & 1) * 8192 + (((q + kr) & 3) * 16 + (lane >> 5) * 8 + dq) * 128 + ((G ^ (dq >> 1)) & 1) * 64 + (lane & 3) * 8;
        wb[q][0] = lbase(b0 + ((lane >> 4) & 1) * 32); wb[q][1] = lbase(b0 + (((lane >> 4) & 1) ^ 1) * 32);
    }
    asm volatile("s_waitcnt vmcnt(6) lgkmcnt(0)\n\ts_barrier" ::: "memory");
    STAMP(4);
    S2_LOAD(5); S2_LOAD(6); S2_LOAD(7);
#define S_FRAG(n) cat4(trd(wb[((n) >> 1) & 3][(n) & 1] + (((n) >> 3) & 3) * 16384), trd(wb[((n) >> 1) & 3][(n) & 1] + (((n) >> 3) & 3) * 16384 + 512))
#define REBASE(d) do { _Pragma("unroll") for (int q = 0; q < 4; ++q) { wb[q][0] = lbase(wb[q][0] + (d)); wb[q][1] = lbase(wb[q][1] + (d)); } } while (0)
#pragma unroll
    for (int m = 0; m < WD; ++m) wf[m] = S_FRAG(m);
    float sd, rs;
    bf16x8 muf;
    {
        const f32x2 sa_ = *(LAS const f32x2*)(smem + L_STAT + tau * 256 + (lane & 31) * 8), sb_ = *(LAS const f32x2*)(smem + L_STAT + 1024 + tau * 256 + (lane & 31) * 8);
        const float mean = (sa_[0] + sb_[0]) * (1.f / 512.f);
        const float var = __builtin_fmaxf((sa_[1] + sb_[1]) * (1.f / 512.f) - mean * mean, 0.f);
        sd = rbf(__builtin_sqrtf(rbf(rbf(var) + 1e-5f)));
        rs = 1.f / sd;
        const unsigned nm = pk2(-mean, -mean);
        muf = PB((u32x4{nm, nm, nm, nm}));
    }
    const lptr tw = smem + L_COMB + w * 1024 + ((lane & 31) >> 2) * 128 + (lane >> 5) * 64 + (lane & 3) * 16;
    const lptr tr = smem + L_COMB + w * 1024 + ((lane >> 4) & 1) * 512 + (lane >> 5) * 128 + ((lane >> 1) & 1) * 64 + dq * 16 + (lane & 1) * 8;
    const unsigned vto = (unsigned)(((lane >> 4) & 1) * 512 + (lane >> 5) * 256 + l15 * 16);
#define QKV_OUT(m, hr, hs) do { \
        const f32x16 a = MFMA32(*(LAS const bf16x8*)(csr + (m) * csi), muf, ac[m]); \
        const unsigned o = WS_Q + (unsigned)((hr) * 64 + tt) * 1024; \
        const float sc = (hr) < 32 ? rs * (0.25f * 1.4426950408889634f) : rs; \
        const u32x4 h0 = pack8(a, 0, sc), h1 = pack8(a, 8, sc); \
        if ((hr) < 64) { \
            __builtin_amdgcn_raw_buffer_store_b128(h0, rs_ws, lin, o, 16); \
            __builtin_amdgcn_raw_buffer_store_b128(h1, rs_ws, lin, o + (hs) * 65536, 16); \
        } else {          \
            *(LAS u32x4*)tw = h0; \
            const bf16x8 f0 = cat4(trd(tr), trd(tr + 256)); \
            *(LAS u32x4*)tw = h1; \
            const bf16x8 f1 = cat4(trd(tr), trd(tr + 256)); \
            __builtin_amdgcn_raw_buffer_store_b128(__builtin_bit_cast(u32x4, f0), rs_ws, vto, o, 16); \
            __builtin_amdgcn_raw_buffer_store_b128(__builtin_bit_cast(u32x4, f1), rs_ws, vto, o + (hs) * 65536, 16); \
        } } while (0)
    const float ns = rs * -1.4426950408889634f;
#define H_OUT(m, fu) do { \
        const f32x16 a = MFMA32(*(LAS const bf16x8*)(csr + (m) * csi), muf, ac[m]); \
        float hv[8]; \
        _Pragma("unroll") for (int e = 0; e < 8; ++e) {       \
            const float ex = __builtin_amdgcn_exp2f(a[e] * ns); \
            hv[e] = a[8 + e] * __builtin_amdgcn_rcpf(__builtin_fmaf(ex, sd, sd)); } \
        const u32x4 pv = {pk2(hv[0], hv[1]), pk2(hv[2], hv[3]), pk2(hv[4], hv[5]), pk2(hv[6], hv[7])}; \
        __builtin_amdgcn_raw_buffer_store_b128(pv, rs_ws, lin, WS_H + (unsigned)(tt * 112 + (fu) * 4 + G * 2 + (m)) * 1024, 16); \
    } while (0)
    QKV_OUT(0, (6 * cq + jl) * 4 + 2 * G, 1);
    STAMP(5);
#define S_STEP(n, LAST) do { \
        ac[(n) & 1] = MFMA32(wf[(n) % WD], PB(xr[(n) >> 1]), ac[(n) & 1]); \
        PIN(ac[(n) & 1]); \
        if constexpr (((n) & 6) == 0) { cs[(n) & 1] = MFMA16(wf[(n) % WD], self, cs[(n) & 1]); PIN(cs[(n) & 1]); } \
        if constexpr ((n) + WD < 64) wf[(n) % WD] = S_FRAG((n) + WD); \
        SB(); } while (0)
    ac[0] = f32x16{}; ac[1] = f32x16{}; cs[0] = f32x4{}; cs[1] = f32x4{};
    SFOR(n, 64)
        if constexpr (n == 16 - WD) { WAITV(8); BAR(); S3_LOAD(0); }
        if constexpr (n == 32 - WD) {
            WAITV(2); BAR();
            if (threadIdx.x == 0) __hip_atomic_store((gu64*)(ws + WS_F1 + ((6 * cq + jl) * 16 + rb) * 8), epoch, RLX_AGENT);
            S3_LOAD(1); S3_LOAD(2); REBASE(65536);
        }
        if constexpr (n == 48 - WD) { BAR(); S3_LOAD(3); S3_LOAD(4); }
        S_STEP(n, 0);
    SEND
    CS_WRITE(0); CS_WRITE(1);
    REBASE(-65536);
    asm volatile("s_waitcnt vmcnt(4) lgkmcnt(0)\n\ts_barrier" ::: "memory");
    STAMP(6);
    S3_LOAD(5); S3_LOAD(6); S3_LOAD(7);
#pragma unroll
    for (int m = 0; m < WD; ++m) wf[m] = S_FRAG(m);
    if (tyb) { QKV_OUT(0, (6 * cq + 4) * 4 + 2 * G, 4); QKV_OUT(1, (6 * cq + 4) * 4 + 2 * G + 1, 4); }
    else { H_OUT(0, fu2); H_OUT(1, fu2); }
    STAMP(7);
    const int head = xcd * 4 + (i >> 3), qblk = i & 7;
    gcptr kvsrc = (gcptr)ws + (G ? WS_V : WS_K + 1024) + (size_t)head * 65536 + (((w >> 1) & 1) * 32 + (w & 1)) * 1024;
#define KV_LOAD(ch) glds16_dev(kvsrc + (ch) * 2048, lin, lds0 + (ch) * 8192 + w * 1024)
    const unsigned f1o = (unsigned)((((lane < 16 ? 8 : lane < 32 ? 16 : 0) + (head >> 2)) * 16 + l15) * 8);
    u64 fl = 0;
    ac[0] = f32x16{}; ac[1] = f32x16{}; cs[0] = f32x4{}; cs[1] = f32x4{};
    SFOR(n, 64)
        if constexpr (n == 16 - WD) {
            WAITV(0); BAR();
            if (tyb && threadIdx.x < 2) __hip_atomic_store((gu64*)(ws + WS_F1 + ((6 * cq + 4 + lane) * 16 + rb) * 8), epoch, RLX_AGENT);
        }
        if constexpr (n == 32 - WD) REBASE(65536);
        if constexpr (n == 44) gload8_dev(fl, (gcptr)ws + WS_F1, f1o);
        S_STEP(n, 0);
    SEND
#undef S_STEP
#undef S_FRAG
#undef REBASE
    CS_WRITE(0); CS_WRITE(1);
    WAITV(0); PIN(fl);
    if (w == 0 && !__all(fl == epoch)) poll_flags((gcptr)ws + WS_F1, f1o, epoch);
    asm volatile("s_waitcnt lgkmcnt(0)\n\ts_barrier" ::: "memory");
    STAMP(8);
    u32x4 qa_, qb_, k0_, kc_;
    {
        gcptr qp = (gcptr)ws + WS_Q + (size_t)(head * 64 + qblk * 8 + tau * 2) * 1024;
        gcptr kp = (gcptr)ws + WS_K + (size_t)head * 65536;
        gload16_dev(qa_, qp, lin); gload16_dev(qb_, qp + 1024, lin); gload16_dev(k0_, kp, lin); gload16_dev(kc_, kp + G * 32768, lin);
    }
#pragma nounroll
    for (int ch = 0; ch < 16; ++ch) KV_LOAD(ch);
#undef KV_LOAD
    H_OUT(0, fu3); H_OUT(1, fu3);
    WAITV(14); BAR();
    PIN(qa_); PIN(qb_); PIN(k0_); PIN(kc_);
    touch(w < 7 ? (gcptr)wd_ + (size_t)w * 262144 : (gcptr)wo_, (unsigned)(i * 64 + lane) * 128, lds0 + L_DUMP);
    STAMP(9);

    const int tb = xcd * 4 + (i >> 3), cb = i & 7;
    const int tt4 = tb * 2 + (w & 1), kq = w >> 1;
    const bf16x8 qa = PB(qa_), qb = PB(qb_);
    bf16x8 kc = PB(kc_);
    f32x16 ca, cb_, sa, sb;
    {
        const f32x16 s = MFMA32(PB(k0_), qa, f32x16{});
        const f32x16 s2 = MFMA32(PB(k0_), qb, f32x16{});
        const float na = -half_swap_max(max16(s)), nb = -half_swap_max(max16(s2));
#pragma unroll
        for (int r = 0; r < 16; ++r) { ca[r] = na; cb_[r] = nb; }
        sa = MFMA32(kc, qa, ca);
    }
    const bool data = (lane & 16) == 0;
    const unsigned vdat = 4096 + G * 2048 + (lane >> 5) * 256 + l15 * 16;
    const unsigned vone = L_ONES + (((l15 + 12) & 8) ? 0 : 64) + ((l15 & 11) ? 16 : 0);
    const unsigned vinc = data ? 8192u : 0u;
    f32x16 oa = {}, ob = {};
    u32x4 p0 = {0u, 0u, 0u, 0u}, p1 = {0u, 0u, 0u, 0u};
    bf16x8 v0, v1 = {0, 0, 0, 0, 0, 0, 0, 0};
    const unsigned bvo = (lane >> 3) * 1024 + sw0;
    gcptr bd = (gcptr)wd_ + w * 8192 + cb * 128, bo = (gcptr)wo_ + w * 8192 + cb * 128;
#define B_POS(n) ((n) % 19)
#define B_LOAD(n) glds16((n) < 28 ? bd + (n) * 65536 : bo + ((((n) - 28) + cb) & 7) * 65536, bvo, lds0 + B_POS(n) * 8192 + w * 1024)
#define EXPR(S, a_, b_) do { _Pragma("unroll") for (int r = (a_); r < (b_); ++r) S[r] = __builtin_amdgcn_exp2f(S[r]); } while (0)
#define HALF(SC, MF_A, MF_B, MF_C, LD_A, LD_B) do { \
        MF_A; SB(); LD_A; EXPR(SC, 0, 5); PIN(SC); SB(); \
        MF_B; SB(); LD_B; EXPR(SC, 5, 8); p0 = pack8n(SC, 0); PIN(p0); SB(); EXPR(SC, 8, 10); PIN(SC); SB(); \
        MF_C; SB(); EXPR(SC, 10, 16); PIN(SC); p1 = pack8n(SC, 8); PIN(p1); SB(); \
    } while (0)
#define TILE(ko, vo) do { \
        HALF(sa, do { sb = MFMA32(kc, qb, cb_); PIN(sb); } while (0), do { ob = MFMA32(v1, PB(p1), ob); PIN(ob); } while (0), do { oa = MFMA32(v0, PB(p0), oa); PIN(oa); } while (0), \
             do { kc = *(LAS const bf16x8*)(kp + (ko)); v0 = *(LAS const bf16x8*)(vp + (vo)); } while (0), v1 = *(LAS const bf16x8*)(vp + (vo) + 512)); \
        HALF(sb, do { sa = MFMA32(kc, qa, ca); PIN(sa); } while (0), do { oa = MFMA32(v1, PB(p1), oa); PIN(oa); } while (0), do { ob = MFMA32(v0, PB(p0), ob); PIN(ob); } while (0), (void)0, (void)0); \
    } while (0)
#define CHUNK(q) do { __builtin_amdgcn_s_setprio(3 - (q)); TILE((q) * 8192, 0); TILE((q) * 8192 + 1024, 1024); vp = lbase(vp + vinc); } while (0)
#define SUPER(pos) do { \
        const lptr kp = lbase(smem + (pos) * 8192 + G * 2048 + lin); \
        lptr vp = lbase(smem + (data ? vdat + (pos) * 8192 : vone)); \
        CHUNK(0); CHUNK(1); CHUNK(2); CHUNK(3); \
    } while (0)
    const unsigned f2o = (unsigned)(((tb >> 1) * 16 + l15) * 8);
#pragma nounroll
    for (int sc = 0; sc < 2; ++sc) {
        SUPER(sc * 4);
        if (sc == 0) {
            asm volatile("s_waitcnt vmcnt(0) lgkmcnt(0)\n\ts_barrier" ::: "memory");
            if (threadIdx.x == 0) __hip_atomic_store((gu64*)(ws + WS_F2 + (rb * 16 + cq * 4 + jl) * 8), epoch, RLX_AGENT);
            B_LOAD(0); B_LOAD(1); B_LOAD(2); B_LOAD(3);
            if (w == 7) touch((gcptr)wo_ + 262144, (unsigned)(i * 64 + lane) * 128, lds0 + L_DUMP);
        }
    }
    if (w == 1 && blockIdx.x == 0) {
#pragma unroll
        for (int q = 0; q < 4; ++q) poll_flags((gcptr)ws + WS_F2, (unsigned)(q * 64 + lane) * 8, epoch);
        if (lane == 0) __hip_atomic_store((gu64*)(ws + WS_N), epoch, RLX_AGENT);
    }
    if (w == 0) poll_flags((gcptr)ws + WS_F2, f2o, epoch);
    asm volatile("s_waitcnt lgkmcnt(0)\n\ts_barrier" ::: "memory");
    B_LOAD(4); B_LOAD(5); B_LOAD(6); B_LOAD(7);
    constexpr int HP = 28;
    u32x4 hf[28];
    gcptr hsrc = (gcptr)ws + WS_H + (size_t)(tt4 * 112 + kq) * 1024;
#pragma unroll
    for (int n = 0; n < HP; ++n) gload16_dev(hf[n], hsrc + n * 4096, lin);
#pragma nounroll
    for (int sc = 2; sc < 4; ++sc) {
        SUPER(sc * 4);
        if (sc == 2) {
            asm volatile("s_waitcnt lgkmcnt(0)\n\ts_barrier" ::: "memory");
            B_LOAD(8); B_LOAD(9); B_LOAD(10); B_LOAD(11);
        }
    }
    ob = MFMA32(v1, PB(p1), ob);
#undef SUPER
#undef CHUNK
#undef TILE
#undef HALF
#undef EXPR
    STAMP(10);
    {
        const f32x16 snd = G ? oa : ob;
        const lptr ex = smem + L_COMB + w * 3072 + lin;
        *(LAS f32x4*)(ex) = f32x4{snd[0], snd[1], snd[2], snd[3]};
        *(LAS f32x4*)(ex + 1024) = f32x4{snd[4], snd[5], snd[6], snd[7]};
        *(LAS float*)(ex + 2048) = snd[8];
    }
    asm volatile("s_waitcnt vmcnt(0) lgkmcnt(0)\n\ts_barrier" ::: "memory");
    {
        f32x16 o = G ? ob : oa;
        const lptr ex = smem + L_COMB + (w ^ 4) * 3072 + lin;
        const f32x4 e0 = *(LAS const f32x4*)(ex), e1 = *(LAS const f32x4*)(ex + 1024);
        const float e2 = *(LAS const float*)(ex + 2048);
#pragma unroll
        for (int r = 0; r < 4; ++r) { o[r] += e0[r]; o[4 + r] += e1[r]; }
        o[8] += e2;
        bool ok = o[8] <= 1.0995116e12f;
#pragma unroll
        for (int r = 0; r < 8; ++r) ok = ok && (__builtin_fabsf(o[r]) <= 3.0e38f);
        if (__builtin_expect(!__all(ok), 0)) attn_slow(rs_ws, head, G ? qb : qa, lane, o);
        const float rl = 1.f / o[8];
        __builtin_amdgcn_raw_buffer_store_b128(pack8(o, 0, rl), rs_ws, lin, WS_O + (unsigned)((qblk * 8 + (w & 3) * 2 + G) * 32 + head) * 1024, 16);
    }
    STAMP(11);
    u32x4 xv;
    const unsigned xo = (unsigned)((tb * 64 + w * 8 + (lane >> 3)) * 1024 + cb * 128 + (lane & 7) * 16);
    B_LOAD(12); B_LOAD(13); B_LOAD(14); B_LOAD(15);
    gload16(xv, (gcptr)x_, xo);
#pragma unroll
    for (int n = 0; n < 28; ++n) PIN(hf[n]);
    lptr db[2][3];
#pragma unroll
    for (int t = 0; t < 2; ++t) {
        db[t][0] = lbase(smem + (kq * 16 + (lane >> 5) * 4 + dq) * 128 + (((dq >> 1) & 1) ^ t) * 64 + ((lane >> 4) & 1) * 32 + (lane & 3) * 8);
        db[t][1] = lbase(db[t][0] + 65536); db[t][2] = lbase(db[t][0] + 131072);
    }
    f32x16 dd[2] = {};
#define D_FRAG(n) cat4(trd(db[(n) & 1][B_POS((n) >> 1) >> 3] + (B_POS((n) >> 1) & 7) * 8192), trd(db[(n) & 1][B_POS((n) >> 1) >> 3] + (B_POS((n) >> 1) & 7) * 8192 + 1024))
#pragma unroll
    for (int m = 0; m < WD; ++m) wf[m] = D_FRAG(m);
#define D_STEP(n) do { dd[(n) & 1] = MFMA32(wf[(n) % WD], PB(hf[(n) >> 1]), dd[(n) & 1]); PIN(dd[(n) & 1]); \
        if ((n) + WD < 56) wf[(n) % WD] = D_FRAG(((n) + WD) % 56); SB(); } while (0)
#define D_STEP4(n) do { D_STEP(n); D_STEP((n) + 1); D_STEP((n) + 2); D_STEP((n) + 3); } while (0)
    D_STEP4(0);
    WAITV(5); BAR();
    if (threadIdx.x == 0) __hip_atomic_store((gu64*)(ws + WS_F3 + (qblk * 32 + head) * 8), epoch, RLX_AGENT);
    B_LOAD(16); B_LOAD(17); B_LOAD(18); B_LOAD(19); B_LOAD(20);
    D_STEP4(4); D_STEP4(8);
    BAR(); B_LOAD(21); B_LOAD(22); B_LOAD(23); B_LOAD(24);
    D_STEP4(12); D_STEP4(16);
    WAITV(9); BAR(); B_LOAD(25); B_LOAD(26); B_LOAD(27); B_LOAD(28);
    PIN(xv);
    D_STEP4(20); D_STEP4(24);
    WAITV(9); BAR(); B_LOAD(29); B_LOAD(30); B_LOAD(31); B_LOAD(32);
    D_STEP4(28); D_STEP4(32);
    WAITV(9); BAR(); B_LOAD(33); B_LOAD(34); B_LOAD(35);
    D_STEP4(36); D_STEP4(40);
    WAITV(8); BAR();
    const unsigned f3o = (unsigned)(((tb >> 2) * 32 + (lane & 31)) * 8);
    gload8_dev(fl, (gcptr)ws + WS_F3, f3o);
    D_STEP4(44); D_STEP4(48); D_STEP4(52);
#undef D_STEP4
#undef D_STEP
#undef D_FRAG
    STAMP(12);
    WAITV(0); PIN(fl);
    if (!__all(fl == epoch)) poll_flags((gcptr)ws + WS_F3, f3o, epoch);
    STAMP(13);
    u32x4 of[8];
    {
        gcptr op = (gcptr)ws + WS_O + (size_t)(tt4 * 32 + kq) * 1024;
#pragma unroll
        for (int m = 0; m < 8; ++m) gload16_dev(of[m], op + ((m + cb) & 7) * 4096, lin);
    }
    BAR();
    bf16x8 wo_f[16];
    {
        lptr ob_[2];
#pragma unroll
        for (int t = 0; t < 2; ++t)
            ob_[t] = lbase(smem + 9 * 8192 + (kq * 16 + (lane >> 5) * 8 + dq) * 128 + (((dq >> 1) & 1) ^ t) * 64 + ((lane >> 4) & 1) * 32 + (lane & 3) * 8);
#pragma unroll
        for (int n = 0; n < 16; ++n) wo_f[n] = cat4(trd(ob_[n & 1] + (n >> 1) * 8192), trd(ob_[n & 1] + (n >> 1) * 8192 + 512));
    }
    const int prow = (w & 1) * 32 + (lane & 31);
    const lptr pw = smem + kq * 16384 + prow * 256;
    const int rrow = w * 8 + (lane >> 3);
    const lptr pr0 = smem + rrow * 256 + (((lane & 7) * 2) ^ (rrow & 7)) * 16, pr1 = smem + rrow * 256 + (((lane & 7) * 2 + 1) ^ (rrow & 7)) * 16;
#define PART_WRITE(A, off) do { _Pragma("unroll") for (int t = 0; t < 2; ++t) _Pragma("unroll") for (int g = 0; g < 4; ++g) \
        *(LAS f32x4*)(pw + (off) + ((t * 8 + g * 2 + (lane >> 5)) ^ (prow & 7)) * 16) = f32x4{A[t][4 * g], A[t][4 * g + 1], A[t][4 * g + 2], A[t][4 * g + 3]}; } while (0)
#define PART_READ(P) ((*(LAS const f32x4*)(P) + *(LAS const f32x4*)((P) + 16384)) + (*(LAS const f32x4*)((P) + 32768) + *(LAS const f32x4*)((P) + 49152)))
    PART_WRITE(dd, 0);
    asm volatile("s_waitcnt lgkmcnt(0)\n\ts_barrier" ::: "memory");
    f32x4 glo = PART_READ(pr0), ghi = PART_READ(pr1);
    f32x16 ss[2] = {};
#define O_STEP(m, N) do { WAITV(N); PIN(of[m]); ss[0] = MFMA32(wo_f[2 * (m)], PB(of[m]), ss[0]); ss[1] = MFMA32(wo_f[2 * (m) + 1], PB(of[m]), ss[1]); PIN(ss[0]); PIN(ss[1]); } while (0)
    O_STEP(0, 7); O_STEP(1, 6); O_STEP(2, 5); O_STEP(3, 4); O_STEP(4, 3); O_STEP(5, 2); O_STEP(6, 1); O_STEP(7, 0);
#undef O_STEP
#pragma unroll
    for (int e = 0; e < 4; ++e) {
        glo[e] = glo[e] * __builtin_amdgcn_rcpf(1.f + __builtin_amdgcn_exp2f(glo[e] * -1.4426950408889634f));
        ghi[e] = ghi[e] * __builtin_amdgcn_rcpf(1.f + __builtin_amdgcn_exp2f(ghi[e] * -1.4426950408889634f));
    }
    STAMP(14);
    PART_WRITE(ss, 73728);
    asm volatile("s_waitcnt lgkmcnt(0)\n\ts_barrier" ::: "memory");
    const lptr ps0 = lbase(pr0 + 73728), ps1 = lbase(pr1 + 73728);
    const f32x4 slo = PART_READ(ps0), shi = PART_READ(ps1);
    const u32x4 ov = {pk2(__builtin_fmaf(slo[0], glo[0], bf_lo(xv[0])), __builtin_fmaf(slo[1], glo[1], bf_hi(xv[0]))),
                      pk2(__builtin_fmaf(slo[2], glo[2], bf_lo(xv[1])), __builtin_fmaf(slo[3], glo[3], bf_hi(xv[1]))),
                      pk2(__builtin_fmaf(shi[0], ghi[0], bf_lo(xv[2])), __builtin_fmaf(shi[1], ghi[1], bf_hi(xv[2]))),
                      pk2(__builtin_fmaf(shi[2], ghi[2], bf_lo(xv[3])), __builtin_fmaf(shi[3], ghi[3], bf_hi(xv[3])))};
    *(GAS u32x4*)((gptr)out_ + xo) = ov;
    STAMP(15);
    asm volatile(".Lcode_end:");
}

extern "C" void kernel_launch(void* const* d_in, const int* in_sizes, int n_in,
                              void* d_out, int out_size, void* d_ws, size_t ws_size,
                              hipStream_t stream) {
    fused_block<<<256, 512, 0, stream>>>((const char*)d_in[0], (const char*)d_in[1], (const char*)d_in[2], (const char*)d_in[4],
                                         (const char*)d_in[5], (const char*)d_in[6], (char*)d_ws, (char*)d_out);
}
```

```cpp
#include <hip/hip_runtime.h>
#include <hip/hip_bf16.h>
#include <stdint.h>

typedef short bf16x8 __attribute__((ext_vector_type(8)));
typedef short s16x4 __attribute__((ext_vector_type(4)));
typedef float f32x16 __attribute__((ext_vector_type(16)));
typedef float f32x4 __attribute__((ext_vector_type(4)));
typedef float f32x2 __attribute__((ext_vector_type(2)));
typedef __bf16 bfx2 __attribute__((ext_vector_type(2)));
typedef unsigned u32x4 __attribute__((ext_vector_type(4)));
typedef unsigned u32x2 __attribute__((ext_vector_type(2)));
typedef unsigned long long u64;
#define LAS __attribute__((address_space(3)))
#define GAS __attribute__((address_space(1)))
typedef LAS char* lptr;
typedef GAS const char* gcptr;
typedef GAS char* gptr;
typedef GAS u64 gu64;

#define MFMA32(a, b, c) __builtin_amdgcn_mfma_f32_32x32x16_bf16((a), (b), (c), 0, 0, 0)
#define MFMA16(a, b, c) __builtin_amdgcn_mfma_f32_16x16x32_bf16((a), (b), (c), 0, 0, 0)
#define RLX_AGENT __ATOMIC_RELAXED, __HIP_MEMORY_SCOPE_AGENT
#define RLX_WG __ATOMIC_RELAXED, __HIP_MEMORY_SCOPE_WORKGROUP
#define PB(x) __builtin_bit_cast(bf16x8, x)

constexpr unsigned WS_Q = 2u << 20;
constexpr unsigned WS_K = 4u << 20;
constexpr unsigned WS_V = 6u << 20;
constexpr unsigned WS_O = 8u << 20;
constexpr unsigned WS_JUNK = 10u << 20;
constexpr unsigned WS_H = 14u << 20;
constexpr unsigned WS_N = 22u << 20;
constexpr unsigned WS_F1 = WS_N + 4096;
constexpr unsigned WS_F2 = WS_N + 8192;
constexpr unsigned WS_F3 = WS_N + 12288;
constexpr unsigned WS_BYTES = 24u << 20;

constexpr int L_COMB = 131072;
constexpr int L_MISC = 155648;
constexpr int L_ONES = L_MISC;
constexpr int L_DUMP = L_MISC + 2048;
constexpr int L_CS = L_MISC + 2304;
constexpr int L_STAT = L_MISC + 4608;

#define STAMP_INIT() do {} while (0)
#define STAMP(n) do {} while (0)
#define STAMP_END() do {} while (0)
#define SB() __builtin_amdgcn_sched_barrier(0)
#define PIN(x) asm volatile("" : "+v"(x))
#define BAR() asm volatile("s_barrier" ::: "memory")
#define WAITV(n) asm volatile("s_waitcnt vmcnt(" #n ")" ::: "memory")
#define WAITL() asm volatile("s_waitcnt lgkmcnt(0)" ::: "memory")
__device__ __forceinline__ unsigned pk2(float lo, float hi) {
    f32x2 v = {lo, hi};
    return __builtin_bit_cast(unsigned, __builtin_convertvector(v, bfx2));
}
__device__ __forceinline__ float bf_lo(unsigned u) { return __uint_as_float(u << 16); }
__device__ __forceinline__ float bf_hi(unsigned u) { return __uint_as_float(u & 0xffff0000u); }
__device__ __forceinline__ float rbf(float f) { return bf_lo(pk2(f, f) & 0xffffu); }
__device__ __forceinline__ void glds16(gcptr sbase, unsigned voff, unsigned lds_base) {
    asm volatile("s_mov_b32 m0, %2\n\ts_nop 0\n\tglobal_load_lds_dwordx4 %0, %1" :: "v"(voff), "s"(sbase), "s"(lds_base) : "memory");
}
__device__ __forceinline__ void glds16_dev(gcptr sbase, unsigned voff, unsigned lds_base) {
    asm volatile("s_mov_b32 m0, %2\n\ts_nop 0\n\tglobal_load_lds_dwordx4 %0, %1 sc1" :: "v"(voff), "s"(sbase), "s"(lds_base) : "memory");
}
__device__ __forceinline__ void touch(gcptr sbase, unsigned voff, unsigned lds_dump) {
    asm volatile("s_mov_b32 m0, %2\n\ts_nop 0\n\tglobal_load_lds_dword %0, %1" :: "v"(voff), "s"(sbase), "s"(lds_dump) : "memory");
}
__device__ __forceinline__ void gload16_dev(u32x4& d, gcptr sbase, unsigned voff) {
    asm volatile("global_load_dwordx4 %0, %1, %2 sc1" : "=v"(d) : "v"(voff), "s"(sbase) : "memory");
}
__device__ __forceinline__ void gload16(u32x4& d, gcptr sbase, unsigned voff) {
    asm volatile("global_load_dwordx4 %0, %1, %2" : "=v"(d) : "v"(voff), "s"(sbase) : "memory");
}
__device__ __forceinline__ void gload8_dev(u64& d, gcptr sbase, unsigned voff) {
    asm volatile("global_load_dwordx2 %0, %1, %2 sc1" : "=v"(d) : "v"(voff), "s"(sbase) : "memory");
}
__device__ __forceinline__ void gstore16_dev(gptr sbase, unsigned voff, u32x4 d) {
    asm volatile("global_store_dwordx4 %0, %1, %2 sc1\n\ts_nop 1" :: "v"(voff), "v"(d), "s"(sbase) : "memory");
}
__device__ __forceinline__ s16x4 trd(lptr p) { return __builtin_amdgcn_ds_read_tr16_b64_v4i16((LAS s16x4*)p); }
__device__ __forceinline__ bf16x8 cat4(s16x4 a, s16x4 b) { return __builtin_shufflevector(a, b, 0, 1, 2, 3, 4, 5, 6, 7); }
__device__ __forceinline__ u32x4 pack8(const f32x16& a, int o, float s) {
    u32x4 r;
    r[0] = pk2(a[o + 0] * s, a[o + 1] * s); r[1] = pk2(a[o + 2] * s, a[o + 3] * s);
    r[2] = pk2(a[o + 4] * s, a[o + 5] * s); r[3] = pk2(a[o + 6] * s, a[o + 7] * s);
    return r;
}
__device__ __forceinline__ u32x4 pack8n(const f32x16& a, int o) {
    u32x4 r;
    r[0] = pk2(a[o + 0], a[o + 1]); r[1] = pk2(a[o + 2], a[o + 3]);
    r[2] = pk2(a[o + 4], a[o + 5]); r[3] = pk2(a[o + 6], a[o + 7]);
    return r;
}
__device__ __forceinline__ float half_swap_add(float v) {
    auto r = __builtin_amdgcn_permlane32_swap(__float_as_uint(v), __float_as_uint(v), false, false);
    return __uint_as_float(r[0]) + __uint_as_float(r[1]);
}
__device__ __forceinline__ float half_swap_max(float v) {
    auto r = __builtin_amdgcn_permlane32_swap(__float_as_uint(v), __float_as_uint(v), false, false);
    return __builtin_fmaxf(__uint_as_float(r[0]), __uint_as_float(r[1]));
}
__device__ __forceinline__ float max16(const f32x16& s) {
    float a = __builtin_fmaxf(__builtin_fmaxf(s[0], s[1]), s[2]), b = __builtin_fmaxf(__builtin_fmaxf(s[3], s[4]), s[5]);
    a = __builtin_fmaxf(__builtin_fmaxf(a, s[6]), s[7]);   b = __builtin_fmaxf(__builtin_fmaxf(b, s[8]), s[9]);
    a = __builtin_fmaxf(__builtin_fmaxf(a, s[10]), s[11]); b = __builtin_fmaxf(__builtin_fmaxf(b, s[12]), s[13]);
    a = __builtin_fmaxf(__builtin_fmaxf(a, s[14]), s[15]);
    return __builtin_fmaxf(a, b);
}
__device__ __forceinline__ lptr lbase(lptr p) { unsigned u = (unsigned)(uintptr_t)p; asm volatile("" : "+v"(u)); return (lptr)(uintptr_t)u; }
__device__ __forceinline__ float dot2(unsigned a, unsigned b, float c) {
    return __builtin_amdgcn_fdot2_f32_bf16(__builtin_bit_cast(bfx2, a), __builtin_bit_cast(bfx2, b), c, false);
}
__device__ __forceinline__ unsigned split_hl(float v) {
    const float hi = rbf(v);
    return pk2(hi, v - hi);
}
__device__ __forceinline__ void poll_flags(gcptr base, unsigned voff, u64 epoch) {
#pragma nounroll
    for (unsigned spins = 0; spins < (1u << 20); ++spins) {
        const u64 v = __hip_atomic_load((GAS const u64*)(base + voff), RLX_AGENT);
        if (__all(v == epoch)) break;
        __builtin_amdgcn_s_sleep(2);
    }
    asm volatile("" ::: "memory");
}

template <int V> struct IC { static constexpr int value = V; };
template <int I, int N, class F> __device__ __forceinline__ void static_for(F&& f) {
    if constexpr (I < N) { f(IC<I>{}); static_for<I + 1, N>(f); }
}
#define SFOR(var, N) static_for<0, (N)>([&](auto ic_##var) __attribute__((always_inline)) { constexpr int var = decltype(ic_##var)::value;
#define SEND });

__device__ __forceinline__ void attn_slow(const __amdgpu_buffer_rsrc_t rs_ws, int head, bf16x8 q, int lane, f32x16& o) {
    float m = -__builtin_inff();
    o = f32x16{};
    const bool data = (lane & 16) == 0;
    const u32x4 ones = {0x3f803f80u, 0x3f803f80u, 0x3f803f80u, 0x3f803f80u};
    const unsigned vl = (unsigned)((lane >> 5) * 256 + (lane & 15) * 16);
#pragma nounroll
    for (int t = 0; t < 64; ++t) {
        const u32x4 k = __builtin_amdgcn_raw_buffer_load_b128(rs_ws, lane * 16, WS_K + head * 65536 + t * 1024, 16);
        u32x4 v0 = __builtin_amdgcn_raw_buffer_load_b128(rs_ws, vl, WS_V + head * 65536 + t * 1024, 16);
        u32x4 v1 = __builtin_amdgcn_raw_buffer_load_b128(rs_ws, vl + 512, WS_V + head * 65536 + t * 1024, 16);
        if (!data) { v0 = ones; v1 = ones; }
        f32x16 s = MFMA32(PB(k), q, f32x16{});
        const float mn = __builtin_fmaxf(m, half_swap_max(max16(s)));
        const float al = __builtin_amdgcn_exp2f(m - mn);
        m = mn;
#pragma unroll
        for (int r = 0; r < 16; ++r) { s[r] = __builtin_amdgcn_exp2f(s[r] - mn); o[r] *= al; }
        o = MFMA32(PB(v0), PB(pack8n(s, 0)), o);
        o = MFMA32(PB(v1), PB(pack8n(s, 8)), o);
    }
}

extern "C" __global__ __launch_bounds__(512, 2) void fused_block(const char* __restrict__ x_, const char* __restrict__ wq_, const char* __restrict__ wo_,
        const char* __restrict__ wg_, const char* __restrict__ wu_, const char* __restrict__ wd_, char* ws_, char* __restrict__ out_) {
    __shared__ __attribute__((aligned(16))) char smem_[163840];
    const int lane = threadIdx.x & 63;
    const int w = __builtin_amdgcn_readfirstlane(threadIdx.x >> 6);
    const int xcd = blockIdx.x & 7, i = blockIdx.x >> 3, G = w >> 2, tau = w & 3;
    const lptr smem = (lptr)smem_;
    const unsigned lds0 = (unsigned)(uintptr_t)smem, lin = lane * 16;
    gptr ws = (gptr)ws_;
    if (w == 7) {
        u64 pc; unsigned len;
        asm volatile("s_getpc_b64 %0\n.Lpc_ref:\n\ts_mov_b32 %1, .Lcode_end-.Lpc_ref-4" : "=s"(pc), "=s"(len));
        const unsigned o = (unsigned)(i * 64 + lane) * 128;
        touch((gcptr)pc, o < len ? o : len, lds0 + L_DUMP);
    }
    STAMP_INIT();
    STAMP(0);
    const int rbl = i >> 2, jl = i & 3, cq = xcd & 3;
    const int rb = (xcd >> 2) * 8 + rbl, tt = rb * 4 + tau, rot = (rbl + 2 * jl) & 7;
    const bool tyb = jl == 3;
    const unsigned sw0 = (unsigned)(((((lane >> 2) ^ (lane >> 4)) & 1) * 64) + (lane & 3) * 16), sw1 = sw0 ^ 32u;
    const unsigned vq = (lane >> 3) * 3072 + sw0;
    gcptr s1p = (gcptr)wq_ + (4 * cq + jl) * 128 + w * 8 * 3072;
#define S1_POS(D) ((D) < 3 ? 16 + (D) : 8 + (D))
#define S1_LOAD(D) glds16(s1p + (size_t)(((D) + rot) & 7) * 64 * 3072, vq, lds0 + S1_POS(D) * 8192 + w * 1024)
    {
        const unsigned v0 = (unsigned)((lane >> 3) * 1024 + (((lane & 7) ^ (lane >> 4)) * 16));
        const unsigned v1 = (unsigned)((8 + (lane >> 3)) * 1024 + (((lane & 7) ^ (4 + (lane >> 4))) * 16));
        gcptr xp = (gcptr)x_ + (size_t)(tt * 32 + G * 16) * 1024;
        const unsigned xd = lds0 + tau * 4096 + G * 2048;
#pragma nounroll
        for (int u = 0; u < 8; ++u) {
            gcptr xs = xp + ((u + rot) & 7) * 128;
            glds16(xs, v0, xd + (u ^ 4) * 16384); glds16(xs, v1, xd + (u ^ 4) * 16384 + 1024);
            if (u == 3) { S1_LOAD(0); S1_LOAD(1); S1_LOAD(2); }
        }
    }
    u64 ep;
    gload8_dev(ep, (gcptr)ws + WS_N, 0u);
    const __amdgpu_buffer_rsrc_t rs_ws = __builtin_amdgcn_make_buffer_rsrc((void*)ws_, 0, (int)WS_BYTES, 0x00020000);
    if (w < 2) {
        const unsigned o = (lane & 1) ? 0u : 0x3f803f80u;
        *(LAS u32x4*)(smem + L_ONES + w * 1024 + lin) = u32x4{o, o, o, o};
    }
    const int p2 = tyb ? 3072 : 3584;
    const unsigned v2a = (lane >> 3) * p2 + sw0, v2b = (lane >> 3) * p2 + sw1, v3a = (lane >> 3) * 3584 + sw0, v3b = (lane >> 3) * 3584 + sw1;
    const int fu2 = 7 * cq + 2 * jl, fu3 = tyb ? fu2 : fu2 + 1;
    gcptr g2a = (tyb ? (gcptr)wq_ + (16 + 2 * cq) * 128 : (gcptr)wg_ + fu2 * 128) + w * 8 * p2;
    gcptr g2b = (tyb ? (gcptr)wq_ + (17 + 2 * cq) * 128 : (gcptr)wu_ + fu2 * 128) + w * 8 * p2;
    gcptr s3a = (gcptr)wg_ + fu3 * 128 + w * 8 * 3584, s3b = (gcptr)wu_ + fu3 * 128 + w * 8 * 3584;
#define S2_LD(e) glds16(((e) & 1 ? g2b : g2a) + (size_t)((((e) >> 1) + rot) & 7) * 64 * p2, (e) & 1 ? v2b : v2a, lds0 + (e) * 8192 + w * 1024)
#define S3_LD(e) glds16(((e) & 1 ? s3b : s3a) + (size_t)((((e) >> 1) + rot) & 7) * 64 * 3584, (e) & 1 ? v3b : v3a, lds0 + (e) * 8192 + w * 1024)
    const int l15 = lane & 15, dq = l15 >> 2, kr = tau + 2 * G;
    u32x4 xr[32];
    lptr xb[4], w1[4], wb[4][2];
#pragma unroll
    for (int q = 0; q < 4; ++q) {
        xb[q] = lbase(smem + 65536 + tau * 4096 + (lane & 31) * 128 + (((2 * ((q + kr) & 3) + (lane >> 5)) ^ ((lane >> 1) & 7)) * 16));
        w1[q] = lbase(smem + 90112 + (((q + kr) & 3) * 16 + (lane >> 5) * 8 + dq) * 128 + ((G ^ (dq >> 1)) & 1) * 64 + ((lane >> 4) & 1) * 32 + (lane & 3) * 8);
    }
    const unsigned selv = l15 == ((lane >> 4) & 1) ? 0x3f803f80u : 0u;
    const bf16x8 self = PB((u32x4{selv, selv, selv, selv}));
    float s1 = 0.f, s2 = 0.f, s1b = 0.f, s2b = 0.f;
#define X_STAT(d) do { const unsigned u_ = xr[((d) >> 3) * 4 + (((d) >> 2) & 1)][(d) & 3]; const float lo_ = bf_lo(u_), hi_ = bf_hi(u_); \
        s1 += lo_; PIN(s1); s2 = __builtin_fmaf(lo_, lo_, s2); PIN(s2); s1b += hi_; PIN(s1b); s2b = __builtin_fmaf(hi_, hi_, s2b); PIN(s2b); } while (0)
    constexpr int WD = 6;
    bf16x8 wf[WD];
    f32x16 a1 = {}, a2[2] = {}, a3[2] = {};
    f32x4 cs[2] = {};
    const lptr csw = smem + L_CS + G * 1024 + ((lane & 1) * 16 + (lane >> 4) * 4) * 16 + tau * 4;
    const lptr csr = smem + (lane < 32 ? L_CS + G * 1024 + lin : L_ONES + 16);
    const unsigned csi = lane < 32 ? 512u : 0u;
#define CS_WRITE(m) do { if (l15 < 2) { _Pragma("unroll") for (int r = 0; r < 4; ++r) *(LAS unsigned*)(csw + (m) * 512 + r * 16) = split_hl(cs[m][r]); } cs[m] = f32x4{}; } while (0)
#define S1_FRAG(n) cat4(trd(w1[(n) & 3] + (S1_POS((n) >> 2) - 11) * 8192), trd(w1[(n) & 3] + (S1_POS((n) >> 2) - 11) * 8192 + 512))
#define S_FRAG(n) cat4(trd(wb[((n) >> 1) & 3][(n) & 1] + (((n) >> 3) & 3) * 16384), trd(wb[((n) >> 1) & 3][(n) & 1] + (((n) >> 3) & 3) * 16384 + 512))
#define REBASE(d) do { _Pragma("unroll") for (int q = 0; q < 4; ++q) { wb[q][0] = lbase(wb[q][0] + (d)); wb[q][1] = lbase(wb[q][1] + (d)); } } while (0)
    WAITV(9); BAR();
    STAMP(1);
#pragma unroll
    for (int m = 0; m < WD; ++m) wf[m] = S1_FRAG(m);
#pragma unroll
    for (int n = 0; n < 16; ++n) xr[n] = *(LAS const u32x4*)(xb[n & 3] + (n >> 2) * 16384);
#pragma unroll
    for (int q = 0; q < 4; ++q) xb[q] = lbase(xb[q] - 65536);
    WAITL(); BAR();
    SFOR(n, 32)
        if constexpr (n == 12) {
            WAITV(0); BAR();
            STAMP(2);
#pragma unroll
            for (int m = 0; m < WD; ++m) wf[m] = S1_FRAG(12 + m);
#pragma unroll
            for (int k = 16; k < 32; ++k) xr[k] = *(LAS const u32x4*)(xb[k & 3] + ((k >> 2) - 4) * 16384);
        }
        if constexpr (n == 14) { WAITL(); BAR(); }
        if constexpr (n == 32 - WD) {
            WAITV(8); BAR();
#pragma unroll
            for (int q = 0; q < 4; ++q) { wb[q][0] = lbase(w1[q] + (((lane >> 4) & 1) * 8192 - 90112)); wb[q][1] = lbase((lptr)((unsigned)(uintptr_t)wb[q][0] ^ 32u)); }
        }
        a1 = MFMA32(wf[n % WD], PB(xr[n]), a1); PIN(a1);
        if constexpr ((n & 3) == 0) { cs[0] = MFMA16(wf[n % WD], self, cs[0]); PIN(cs[0]); }
        if constexpr (n + WD < 12 || (n >= 12 && n + WD < 32)) wf[n % WD] = S1_FRAG(n + WD);
        if constexpr (n + WD >= 32) wf[n % WD] = S_FRAG(n + WD - 32);
        if constexpr (n < 5) S1_LOAD(3 + n);
        if constexpr (n >= 14 && n < 24) S2_LD(n - 14);
        if constexpr (n < 14) { X_STAT(n * 16 / 7); X_STAT(n * 16 / 7 + 1); if constexpr ((n + 1) * 16 / 7 - n * 16 / 7 > 2) X_STAT(n * 16 / 7 + 2); }
        if constexpr (n >= 14 && n < 30) { X_STAT(32 + (n - 14) * 2); X_STAT(33 + (n - 14) * 2); }
        if constexpr (n == 30) CS_WRITE(0);
        if constexpr (n == 31) *(LAS f32x2*)(smem + L_STAT + w * 256 + (lane & 31) * 8) = f32x2{half_swap_add(s1 + s1b), half_swap_add(s2 + s2b)};
        SB();
    SEND
#undef X_STAT
#undef S1_FRAG
    STAMP(4);
    PIN(ep);
    const u64 epoch = (((u64)(unsigned)__builtin_amdgcn_readfirstlane((unsigned)(ep >> 32)) << 32) | (unsigned)__builtin_amdgcn_readfirstlane((unsigned)ep)) + 1ull;
#define WSL(sw, n) (((sw) == 2 ? 32 + (n) : 96 + (n)) % WD)
#define S_STEP(A, sw, n) do { \
        A[(n) & 1] = MFMA32(wf[WSL(sw, n)], PB(xr[(n) >> 1]), A[(n) & 1]); \
        PIN(A[(n) & 1]); \
        if constexpr (((n) & 6) == 2) { cs[(n) & 1] = MFMA16(wf[WSL(sw, n)], self, cs[(n) & 1]); PIN(cs[(n) & 1]); } \
        if constexpr ((n) + WD < 64 || (sw) == 2) wf[WSL(sw, n)] = S_FRAG(((n) + WD) & 63); } while (0)
    f32x2 sta = {0.f, 0.f}, stb = {0.f, 0.f};
    bf16x8 cf0 = {};
    float sd = 0.f, rs = 0.f, ns = 0.f, sc1 = 0.f;
    u32x4 mu4 = {0u, 0u, 0u, 0u};
    const int u1 = 4 * cq + jl;
    const unsigned o1 = WS_Q + (unsigned)((u1 * 4 + 2 * G) * 64 + tt) * 1024;
    SFOR(n, 64)
        if constexpr (n == 8 - WD) {
            asm volatile("s_waitcnt vmcnt(4) lgkmcnt(4)\n\ts_barrier" ::: "memory");
            sta = *(LAS const f32x2*)(smem + L_STAT + tau * 256 + (lane & 31) * 8); stb = *(LAS const f32x2*)(smem + L_STAT + 1024 + tau * 256 + (lane & 31) * 8);
            cf0 = *(LAS const bf16x8*)(csr);
        }
        if constexpr (n == 24 - WD) { WAITV(8); BAR(); }
        if constexpr (n == 32 - WD) REBASE(65536);
        if constexpr (n == 40 - WD) {
            WAITV(4); BAR();
            if (threadIdx.x == 0) __hip_atomic_store((gu64*)(ws + WS_F1 + (u1 * 16 + rb) * 8), epoch, RLX_AGENT);
        }
        if constexpr (n == 50) { WAITV(4); BAR(); }
        if constexpr (n == 64 - WD) REBASE(-65536);
        S_STEP(a2, 2, n);
        if constexpr (n >= 2 && n < 8) S2_LD(8 + n);
        if constexpr (n >= 18 && n < 22) S3_LD(n - 18);
        if constexpr (n >= 34 && n < 38) S3_LD(n - 30);
        if constexpr (n >= 50 && n < 54) S3_LD(n - 42);
        if constexpr (n == 8) {
            const float mean = (sta[0] + stb[0]) * (1.f / 512.f);
            const float var = __builtin_fmaxf((sta[1] + stb[1]) * (1.f / 512.f) - mean * mean, 0.f);
            const unsigned nm = pk2(-mean, -mean);
            mu4 = u32x4{nm, nm, nm, nm};
            sd = rbf(rbf(var) + 1e-5f);
            PIN(mu4); PIN(sd);
        }
        if constexpr (n == 9) {
            a1 = MFMA32(cf0, PB(mu4), a1); PIN(a1);
            sd = rbf(__builtin_amdgcn_sqrtf(sd));
            rs = __builtin_amdgcn_rcpf(sd);
            ns = rs * -1.4426950408889634f;
            sc1 = u1 < 8 ? rs * (0.25f * 1.4426950408889634f) : rs;
            PIN(sd); PIN(rs); PIN(ns); PIN(sc1);
        }
        if constexpr (n == 11) gstore16_dev(ws + o1, lin, pack8(a1, 0, sc1));
        if constexpr (n == 13) gstore16_dev(ws + (o1 + 65536), lin, pack8(a1, 8, sc1));
        if constexpr (n == 60) CS_WRITE(0);
        if constexpr (n == 61) CS_WRITE(1);
        SB();
    SEND
    asm volatile("s_waitcnt vmcnt(4) lgkmcnt(4)\n\ts_barrier" ::: "memory");
    STAMP(6);
    if (tyb) {
        int lane = (int)(lin >> 4);
        PIN(lane);
        const int l15 = lane & 15, dq = l15 >> 2;
        const lptr tw = smem + L_COMB + w * 2048 + ((lane & 31) >> 2) * 128 + (lane >> 5) * 64 + (lane & 3) * 16;
        const lptr tr = smem + L_COMB + w * 2048 + ((lane >> 4) & 1) * 512 + (lane >> 5) * 128 + ((lane >> 1) & 1) * 64 + dq * 16 + (lane & 1) * 8;
        const unsigned vto = (unsigned)(((lane >> 4) & 1) * 512 + (lane >> 5) * 256 + l15 * 16);
#pragma unroll
        for (int m = 0; m < 2; ++m) {
            a2[m] = MFMA32(*(LAS const bf16x8*)(csr + m * csi), PB(mu4), a2[m]);
            const unsigned o = WS_V + (unsigned)((cq * 8 + 2 * G + m) * 64 + tt) * 1024;
            *(LAS u32x4*)tw = pack8(a2[m], 0, rs); *(LAS u32x4*)(tw + 1024) = pack8(a2[m], 8, rs);
            const bf16x8 f0 = cat4(trd(tr), trd(tr + 256)), f1 = cat4(trd(tr + 1024), trd(tr + 1280));
            gstore16_dev(ws + o, vto, __builtin_bit_cast(u32x4, f0));
            gstore16_dev(ws + (o + 4 * 65536), vto, __builtin_bit_cast(u32x4, f1));
        }
    }
    STAMP(7);
    const int head = xcd * 4 + (i >> 3), qblk = i & 7;
    gcptr kvsrc = (gcptr)ws + (G ? WS_V : WS_K) + (size_t)head * 65536 + ((w >> 1) & 1) * 32768;
    const int kv0 = qblk * 4 + (w & 1) + 1 - G;
#define KV_LOAD(ch) glds16_dev(kvsrc + ((kv0 + 2 * (ch)) & 31) * 1024, lin, lds0 + (ch) * 8192 + w * 1024)
    const unsigned f1o = (unsigned)((((G ? 16 : lane < 32 ? 8 : 0) + (head >> 2)) * 16 + l15) * 8);
    u64 fl = 0;
    bool vearly = false;
#define H_ELT(A, e) A[e] = A[8 + (e)] * __builtin_amdgcn_rcpf(__builtin_fmaf(__builtin_amdgcn_exp2f(A[e] * ns), sd, sd))
    const unsigned oh2 = tyb ? WS_JUNK + (unsigned)(blockIdx.x * 8 + w) * 2048 : WS_H + (unsigned)(tt * 112 + fu2 * 4 + G * 2) * 1024;
    SFOR(n, 64)
        if constexpr (n == 12) {
            WAITV(4); BAR();
            if (tyb && threadIdx.x < 2) __hip_atomic_store((gu64*)(ws + WS_F1 + ((16 + 2 * cq + lane) * 16 + rb) * 8), epoch, RLX_AGENT);
        }
        if constexpr (n == 24) { if (!G) gload8_dev(fl, (gcptr)ws + WS_F1, f1o); }
        if constexpr (n == 32 - WD) REBASE(65536);
        if constexpr (n == 36) {
            if (!G) { WAITV(0); PIN(fl); if (!__all(fl == epoch)) poll_flags((gcptr)ws + WS_F1, f1o, epoch); }
            WAITV(2); BAR();
            if (!G) { KV_LOAD(0); KV_LOAD(1); KV_LOAD(2); KV_LOAD(3); }
        }
        if constexpr (n == 46) { if (G) gload8_dev(fl, (gcptr)ws + WS_F1, f1o); }
        if constexpr (n == 56) {
            if (G) { WAITV(0); PIN(fl); vearly = __all(fl == epoch); if (vearly) { KV_LOAD(0); KV_LOAD(1); KV_LOAD(2); KV_LOAD(3); } }
        }
        S_STEP(a3, 3, n);
        if constexpr (n < 4) S3_LD(12 + n);
        if constexpr (n == 2) cf0 = *(LAS const bf16x8*)(csr);
        if constexpr (n == 5) { a2[0] = MFMA32(cf0, PB(mu4), a2[0]); PIN(a2[0]); cf0 = *(LAS const bf16x8*)(csr + csi); }
        if constexpr (n == 9) { a2[1] = MFMA32(cf0, PB(mu4), a2[1]); PIN(a2[1]); }
        if constexpr (n >= 7 && n < 23) { H_ELT(a2[(n - 7) >> 3], (n - 7) & 7); PIN(a2[(n - 7) >> 3]); }
        if constexpr (n == 15) gstore16_dev(ws + oh2, lin, pack8n(a2[0], 0));
        if constexpr (n == 23) gstore16_dev(ws + (oh2 + 1024), lin, pack8n(a2[1], 0));
        if constexpr (n == 60) CS_WRITE(0);
        if constexpr (n == 61) CS_WRITE(1);
        SB();
    SEND
#undef S_STEP
#undef S_FRAG
#undef REBASE
    u32x4 qa_, qb_, k0_, kc_;
    {
        gcptr qp = (gcptr)ws + WS_Q + (size_t)(head * 64 + qblk * 8 + tau * 2) * 1024;
        gcptr kp = (gcptr)ws + WS_K + (size_t)head * 65536;
        gload16_dev(qa_, qp, lin); gload16_dev(qb_, qp + 1024, lin); gload16_dev(k0_, kp, lin); gload16_dev(kc_, kp + G * 32768 + qblk * 4096, lin);
    }
    asm volatile("s_waitcnt lgkmcnt(0)\n\ts_barrier" ::: "memory");
    STAMP(8);
    if (G && !vearly) { poll_flags((gcptr)ws + WS_F1, f1o, epoch); KV_LOAD(0); KV_LOAD(1); KV_LOAD(2); KV_LOAD(3); }
    a3[0] = MFMA32(*(LAS const bf16x8*)(csr), PB(mu4), a3[0]); a3[1] = MFMA32(*(LAS const bf16x8*)(csr + csi), PB(mu4), a3[1]);
#pragma unroll
    for (int m = 0; m < 2; ++m) {
#pragma unroll
        for (int e = 0; e < 8; ++e) H_ELT(a3[m], e);
        gstore16_dev(ws + (WS_H + (unsigned)(tt * 112 + fu3 * 4 + G * 2 + m) * 1024), lin, pack8n(a3[m], 0));
    }
#undef H_ELT
    STAMP(37);
    WAITV(2); BAR();
    STAMP(38);
    PIN(qa_); PIN(qb_); PIN(k0_); PIN(kc_);
#pragma nounroll
    for (int ch = 4; ch < 16; ++ch) KV_LOAD(ch);
#undef KV_LOAD
    int lane2 = (int)(lin >> 4);
    PIN(lane2);
    const int l15b = lane2 & 15;
    touch(w < 7 ? (gcptr)wd_ + (size_t)w * 262144 : (gcptr)wo_, (unsigned)(i * 64 + lane2) * 128, lds0 + L_DUMP);
    STAMP(9);

    const int tb = xcd * 4 + (i >> 3), cb = i & 7;
    const int tt4 = tb * 2 + (w & 1), kq = w >> 1;
    const bf16x8 qa = PB(qa_), qb = PB(qb_);
    bf16x8 kc = PB(kc_);
    f32x16 ca, cb_, sa, sb;
    {
        const f32x16 s = MFMA32(PB(k0_), qa, f32x16{});
        const f32x16 s2 = MFMA32(PB(k0_), qb, f32x16{});
        const float na = -half_swap_max(max16(s)), nb = -half_swap_max(max16(s2));
#pragma unroll
        for (int r = 0; r < 16; ++r) { ca[r] = na; cb_[r] = nb; }
        sa = MFMA32(kc, qa, ca);
    }
    const bool data = (lane2 & 16) == 0;
    const unsigned vdat = 4096 + G * 2048 + (lane2 >> 5) * 256 + l15b * 16;
    const unsigned vone = L_ONES + (((l15b + 12) & 8) ? 0 : 64) + ((l15b & 11) ? 16 : 0);
    const unsigned vinc = data ? 8192u : 0u;
    f32x16 oa = {}, ob = {};
    u32x4 p0 = {0u, 0u, 0u, 0u}, p1 = {0u, 0u, 0u, 0u};
    bf16x8 v0, v1 = {0, 0, 0, 0, 0, 0, 0, 0};
    const unsigned bvo = (lane2 >> 3) * 1024 + ((((lane2 >> 2) ^ (lane2 >> 4)) & 1) * 64) + (lane2 & 3) * 16;
    gcptr bd = (gcptr)wd_ + w * 8192 + cb * 128, bo = (gcptr)wo_ + w * 8192 + cb * 128;
#define B_POS(n) ((n) % 19)
#define B_LOAD(n) glds16((n) < 28 ? bd + (n) * 65536 : bo + ((((n) - 28) + cb) & 7) * 65536, bvo, lds0 + B_POS(n) * 8192 + w * 1024)
#define EXPR(S, a_, b_) do { _Pragma("unroll") for (int r = (a_); r < (b_); ++r) S[r] = __builtin_amdgcn_exp2f(S[r]); } while (0)
#define HALF(SC, MF_A, MF_B, MF_C, LD_A, LD_B) do { \
        MF_A; SB(); LD_A; EXPR(SC, 0, 5); PIN(SC); SB(); \
        MF_B; SB(); LD_B; EXPR(SC, 5, 8); p0 = pack8n(SC, 0); PIN(p0); SB(); EXPR(SC, 8, 10); PIN(SC); SB(); \
        MF_C; SB(); EXPR(SC, 10, 16); PIN(SC); p1 = pack8n(SC, 8); PIN(p1); SB(); \
    } while (0)
#define TILE(ko, vo) do { \
        HALF(sa, do { sb = MFMA32(kc, qb, cb_); PIN(sb); } while (0), do { ob = MFMA32(v1, PB(p1), ob); PIN(ob); } while (0), do { oa = MFMA32(v0, PB(p0), oa); PIN(oa); } while (0), \
             do { kc = *(LAS const bf16x8*)(kp + (ko)); v0 = *(LAS const bf16x8*)(vp + (vo)); } while (0), v1 = *(LAS const bf16x8*)(vp + (vo) + 512)); \
        HALF(sb, do { sa = MFMA32(kc, qa, ca); PIN(sa); } while (0), do { oa = MFMA32(v1, PB(p1), oa); PIN(oa); } while (0), do { ob = MFMA32(v0, PB(p0), ob); PIN(ob); } while (0), (void)0, (void)0); \
    } while (0)
#define CHUNK(q) do { __builtin_amdgcn_s_setprio(3 - (q)); TILE((q) * 8192, 0); TILE((q) * 8192 + 1024, 1024); vp = lbase(vp + vinc); } while (0)
#define SUPER(pos) do { \
        const lptr kp = lbase(smem + (pos) * 8192 + G * 2048 + lin); \
        lptr vp = lbase(smem + (data ? vdat + (pos) * 8192 : vone)); \
        CHUNK(0); CHUNK(1); CHUNK(2); CHUNK(3); \
    } while (0)
    const unsigned f2o = (unsigned)(((tb >> 1) * 16 + l15b) * 8);
#pragma nounroll
    for (int sc = 0; sc < 2; ++sc) {
        SUPER(sc * 4);
        if (sc == 0) {
            asm volatile("s_waitcnt vmcnt(0) lgkmcnt(0)\n\ts_barrier" ::: "memory");
            if (threadIdx.x == 0) __hip_atomic_store((gu64*)(ws + WS_F2 + (rb * 16 + cq * 4 + jl) * 8), epoch, RLX_AGENT);
            B_LOAD(0); B_LOAD(1); B_LOAD(2); B_LOAD(3);
            if (w == 7) touch((gcptr)wo_ + 262144, (unsigned)(i * 64 + lane2) * 128, lds0 + L_DUMP);
        }
    }
    if (w == 1 && blockIdx.x == 0) {
#pragma unroll
        for (int q = 0; q < 4; ++q) poll_flags((gcptr)ws + WS_F2, (unsigned)(q * 64 + lane2) * 8, epoch);
        if (lane2 == 0) __hip_atomic_store((gu64*)(ws + WS_N), epoch, RLX_AGENT);
    }
    if (w == 0) poll_flags((gcptr)ws + WS_F2, f2o, epoch);
    asm volatile("s_waitcnt lgkmcnt(0)\n\ts_barrier" ::: "memory");
    B_LOAD(4); B_LOAD(5); B_LOAD(6); B_LOAD(7);
    constexpr int HP = 28;
    u32x4 hf[28];
    gcptr hsrc = (gcptr)ws + WS_H + (size_t)(tt4 * 112 + kq) * 1024;
#pragma unroll
    for (int n = 0; n < HP; ++n) gload16_dev(hf[n], hsrc + n * 4096, lin);
#pragma nounroll
    for (int sc = 2; sc < 4; ++sc) {
        SUPER(sc * 4);
        if (sc == 2) {
            asm volatile("s_waitcnt lgkmcnt(0)\n\ts_barrier" ::: "memory");
            B_LOAD(8); B_LOAD(9); B_LOAD(10); B_LOAD(11);
        }
    }
    ob = MFMA32(v1, PB(p1), ob);
#undef SUPER
#undef CHUNK
#undef TILE
#undef HALF
#undef EXPR
    STAMP(10);
    {
        const f32x16 snd = G ? oa : ob;
        const lptr ex = smem + L_COMB + w * 3072 + lin;
        *(LAS f32x4*)(ex) = f32x4{snd[0], snd[1], snd[2], snd[3]};
        *(LAS f32x4*)(ex + 1024) = f32x4{snd[4], snd[5], snd[6], snd[7]};
        *(LAS float*)(ex + 2048) = snd[8];
    }
    asm volatile("s_waitcnt vmcnt(0) lgkmcnt(0)\n\ts_barrier" ::: "memory");
    {
        STAMP(36);
    f32x16 o = G ? ob : oa;
        const lptr ex = smem + L_COMB + (w ^ 4) * 3072 + lin;
        const f32x4 e0 = *(LAS const f32x4*)(ex), e1 = *(LAS const f32x4*)(ex + 1024);
        const float e2 = *(LAS const float*)(ex + 2048);
#pragma unroll
        for (int r = 0; r < 4; ++r) { o[r] += e0[r]; o[4 + r] += e1[r]; }
        o[8] += e2;
        bool ok = o[8] <= 1.0995116e12f;
#pragma unroll
        for (int r = 0; r < 8; ++r) ok = ok && (__builtin_fabsf(o[r]) <= 3.0e38f);
        if (__builtin_expect(!__all(ok), 0)) attn_slow(rs_ws, head, G ? qb : qa, lane2, o);
        const float rl = 1.f / o[8];
        gstore16_dev(ws + (WS_O + (unsigned)((qblk * 8 + (w & 3) * 2 + G) * 32 + head) * 1024), lin, pack8(o, 0, rl));
    }
    STAMP(11);
    u32x4 xv;
    PIN(lane2);
    const int dqb = (lane2 & 15) >> 2;
    const unsigned xo = (unsigned)((tb * 64 + w * 8 + (lane2 >> 3)) * 1024 + cb * 128 + (lane2 & 7) * 16);
    B_LOAD(12); B_LOAD(13); B_LOAD(14); B_LOAD(15);
    gload16(xv, (gcptr)x_, xo);
#pragma unroll
    for (int n = 0; n < 28; ++n) PIN(hf[n]);
    lptr db[2][3];
#pragma unroll
    for (int t = 0; t < 2; ++t) {
        db[t][0] = lbase(smem + (kq * 16 + (lane2 >> 5) * 4 + dqb) * 128 + (((dqb >> 1) & 1) ^ t) * 64 + ((lane2 >> 4) & 1) * 32 + (lane2 & 3) * 8);
        db[t][1] = lbase(db[t][0] + 65536); db[t][2] = lbase(db[t][0] + 131072);
    }
    f32x16 dd[2] = {};
#define D_FRAG(n) cat4(trd(db[(n) & 1][B_POS((n) >> 1) >> 3] + (B_POS((n) >> 1) & 7) * 8192), trd(db[(n) & 1][B_POS((n) >> 1) >> 3] + (B_POS((n) >> 1) & 7) * 8192 + 1024))
#pragma unroll
    for (int m = 0; m < WD; ++m) wf[m] = D_FRAG(m);
#define D_STEP(n) do { dd[(n) & 1] = MFMA32(wf[(n) % WD], PB(hf[(n) >> 1]), dd[(n) & 1]); PIN(dd[(n) & 1]); \
        if ((n) + WD < 56) wf[(n) % WD] = D_FRAG(((n) + WD) % 56); SB(); } while (0)
#define D_STEPL(n, b) do { D_STEP(n); B_LOAD(b); } while (0)
#define D_STEP2(n) do { D_STEP(n); D_STEP((n) + 1); } while (0)
#define D_STEP4(n) do { D_STEP2(n); D_STEP2((n) + 2); } while (0)
#define D_GROUP(n, b) do { D_STEPL(n, b); D_STEPL((n) + 1, (b) + 1); D_STEPL((n) + 2, (b) + 2); D_STEPL((n) + 3, (b) + 3); D_STEP4((n) + 4); } while (0)
    const unsigned f3o = (unsigned)(((tb >> 2) * 32 + (lane2 & 31)) * 8);
    STAMP(16);
    D_STEP2(0);
    BAR();
    STAMP(17);
    D_GROUP(2, 16);
    STAMP(18);
    WAITV(9); BAR();
    if (threadIdx.x == 0) __hip_atomic_store((gu64*)(ws + WS_F3 + (qblk * 32 + head) * 8), epoch, RLX_AGENT);
    STAMP(19);
    D_GROUP(10, 20);
    STAMP(20);
    WAITV(8); BAR(); PIN(xv);
    STAMP(21);
    D_GROUP(18, 24);
    STAMP(22);
    WAITV(8); BAR();
    STAMP(23);
    D_GROUP(26, 28);
    STAMP(24);
    WAITV(8); BAR();
    STAMP(25);
    D_GROUP(34, 32);
    STAMP(26);
    gload8_dev(fl, (gcptr)ws + WS_F3, f3o);
    WAITV(9); BAR();
    STAMP(27);
    D_STEP4(42); D_STEP2(46);
    STAMP(28);
    WAITV(0); PIN(fl);
    const bool oearly = __all(fl == epoch);
    STAMP(29);
    u32x4 of[8];
    gcptr op = (gcptr)ws + WS_O + (size_t)(tt4 * 32 + kq) * 1024;
#define O_LOADS() do { _Pragma("unroll") for (int m = 0; m < 8; ++m) gload16_dev(of[m], op + ((m + cb) & 7) * 4096, lin); } while (0)
    if (oearly) { O_LOADS(); D_STEP4(48); D_STEP4(52); }
    else { D_STEP4(48); D_STEP4(52); poll_flags((gcptr)ws + WS_F3, f3o, epoch); O_LOADS(); }
#undef O_LOADS
#undef D_STEP4
#undef D_STEP2
#undef D_STEPL
#undef D_GROUP
#undef D_STEP
#undef D_FRAG
    STAMP(13);
    BAR();
    STAMP(30);
    bf16x8 wo_f[16];
    {
        lptr ob_[2];
#pragma unroll
        for (int t = 0; t < 2; ++t)
            ob_[t] = lbase(smem + 9 * 8192 + (kq * 16 + (lane2 >> 5) * 8 + dqb) * 128 + (((dqb >> 1) & 1) ^ t) * 64 + ((lane2 >> 4) & 1) * 32 + (lane2 & 3) * 8);
#pragma unroll
        for (int n = 0; n < 16; ++n) wo_f[n] = cat4(trd(ob_[n & 1] + (n >> 1) * 8192), trd(ob_[n & 1] + (n >> 1) * 8192 + 512));
    }
    const int prow = (w & 1) * 32 + (lane2 & 31);
    const lptr pw = smem + kq * 16384 + prow * 256;
    const int rrow = w * 8 + (lane2 >> 3);
    const lptr pr0 = smem + rrow * 256 + (((lane2 & 7) * 2) ^ (rrow & 7)) * 16, pr1 = smem + rrow * 256 + (((lane2 & 7) * 2 + 1) ^ (rrow & 7)) * 16;
#define PART_WRITE(A, off) do { _Pragma("unroll") for (int t = 0; t < 2; ++t) _Pragma("unroll") for (int g = 0; g < 4; ++g) \
        *(LAS f32x4*)(pw + (off) + ((t * 8 + g * 2 + (lane2 >> 5)) ^ (prow & 7)) * 16) = f32x4{A[t][4 * g], A[t][4 * g + 1], A[t][4 * g + 2], A[t][4 * g + 3]}; } while (0)
#define PART_READ(P) ((*(LAS const f32x4*)(P) + *(LAS const f32x4*)((P) + 16384)) + (*(LAS const f32x4*)((P) + 32768) + *(LAS const f32x4*)((P) + 49152)))
    PART_WRITE(dd, 0);
    STAMP(31);
    asm volatile("s_waitcnt lgkmcnt(0)\n\ts_barrier" ::: "memory");
    STAMP(32);
    f32x4 glo = PART_READ(pr0), ghi = PART_READ(pr1);
    f32x16 ss[2] = {};
#define O_STEP(m, N) do { WAITV(N); PIN(of[m]); ss[0] = MFMA32(wo_f[2 * (m)], PB(of[m]), ss[0]); ss[1] = MFMA32(wo_f[2 * (m) + 1], PB(of[m]), ss[1]); PIN(ss[0]); PIN(ss[1]); } while (0)
    O_STEP(0, 7); STAMP(33); O_STEP(1, 6); O_STEP(2, 5); O_STEP(3, 4); O_STEP(4, 3); O_STEP(5, 2); O_STEP(6, 1); O_STEP(7, 0);
#undef O_STEP
    STAMP(34);
#pragma unroll
    for (int e = 0; e < 4; ++e) {
        glo[e] = glo[e] * __builtin_amdgcn_rcpf(1.f + __builtin_amdgcn_exp2f(glo[e] * -1.4426950408889634f));
        ghi[e] = ghi[e] * __builtin_amdgcn_rcpf(1.f + __builtin_amdgcn_exp2f(ghi[e] * -1.4426950408889634f));
    }
    STAMP(14);
    PART_WRITE(ss, 73728);
    asm volatile("s_waitcnt lgkmcnt(0)\n\ts_barrier" ::: "memory");
    STAMP(35);
    const lptr ps0 = lbase(pr0 + 73728), ps1 = lbase(pr1 + 73728);
    const f32x4 slo = PART_READ(ps0), shi = PART_READ(ps1);
    const u32x4 ov = {pk2(__builtin_fmaf(slo[0], glo[0], bf_lo(xv[0])), __builtin_fmaf(slo[1], glo[1], bf_hi(xv[0]))),
                      pk2(__builtin_fmaf(slo[2], glo[2], bf_lo(xv[1])), __builtin_fmaf(slo[3], glo[3], bf_hi(xv[1]))),
                      pk2(__builtin_fmaf(shi[0], ghi[0], bf_lo(xv[2])), __builtin_fmaf(shi[1], ghi[1], bf_hi(xv[2]))),
                      pk2(__builtin_fmaf(shi[2], ghi[2], bf_lo(xv[3])), __builtin_fmaf(shi[3], ghi[3], bf_hi(xv[3])))};
    *(GAS u32x4*)((gptr)out_ + xo) = ov;
    STAMP(15);
    STAMP_END();
    asm volatile(".Lcode_end:");
}

extern "C" void kernel_launch(void* const* d_in, const int* in_sizes, int n_in,
                              void* d_out, int out_size, void* d_ws, size_t ws_size,
                              hipStream_t stream) {
    fused_block<<<256, 512, 0, stream>>>((const char*)d_in[0], (const char*)d_in[1], (const char*)d_in[2], (const char*)d_in[4],
                                         (const char*)d_in[5], (const char*)d_in[6], (char*)d_ws, (char*)d_out);
}
```

```cpp
#include <hip/hip_runtime.h>
#include <hip/hip_bf16.h>
#include <stdint.h>

typedef short bf16x8 __attribute__((ext_vector_type(8)));
typedef short s16x4 __attribute__((ext_vector_type(4)));
typedef float f32x16 __attribute__((ext_vector_type(16)));
typedef float f32x4 __attribute__((ext_vector_type(4)));
typedef float f32x2 __attribute__((ext_vector_type(2)));
typedef __bf16 bfx2 __attribute__((ext_vector_type(2)));
typedef unsigned u32x4 __attribute__((ext_vector_type(4)));
typedef unsigned long long u64;
#define LAS __attribute__((address_space(3)))
#define GAS __attribute__((address_space(1)))
typedef LAS char* lptr;
typedef GAS const char* gcptr;
typedef GAS char* gptr;
typedef GAS u64 gu64;

#define MFMA32(a, b, c) __builtin_amdgcn_mfma_f32_32x32x16_bf16((a), (b), (c), 0, 0, 0)
#define MFMA16(a, b, c) __builtin_amdgcn_mfma_f32_16x16x32_bf16((a), (b), (c), 0, 0, 0)
#define RLX_AGENT __ATOMIC_RELAXED, __HIP_MEMORY_SCOPE_AGENT
#define RLX_WG __ATOMIC_RELAXED, __HIP_MEMORY_SCOPE_WORKGROUP
#define PB(x) __builtin_bit_cast(bf16x8, x)

constexpr unsigned WS_Q = 2u << 20;
constexpr unsigned WS_K = 4u << 20;
constexpr unsigned WS_V = 6u << 20;
constexpr unsigned WS_O = 8u << 20;
constexpr unsigned WS_H = 14u << 20;
constexpr unsigned WS_N = 22u << 20;
constexpr unsigned WS_F1 = WS_N + 4096;
constexpr unsigned WS_F2 = WS_N + 8192;
constexpr unsigned WS_F3 = WS_N + 12288;
constexpr unsigned WS_BYTES = 24u << 20;

constexpr int L_COMB = 131072;
constexpr int L_EXCH = 7 * 8192;
constexpr int L_MISC = 155648;
constexpr int L_ONES = L_MISC;
constexpr int L_DUMP = L_MISC + 2048;
constexpr int L_CS = L_MISC + 2304;
constexpr int L_STAT = L_MISC + 5376;
constexpr int L_CNT = L_MISC + 7424;

#define SB() __builtin_amdgcn_sched_barrier(0)
#define PIN(x) asm volatile("" : "+v"(x))
#define BAR() asm volatile("s_barrier" ::: "memory")
#define WAITV(n) asm volatile("s_waitcnt vmcnt(" #n ")" ::: "memory")
__device__ __forceinline__ unsigned pk2(float lo, float hi) {
    f32x2 v = {lo, hi};
    return __builtin_bit_cast(unsigned, __builtin_convertvector(v, bfx2));
}
__device__ __forceinline__ float bf_lo(unsigned u) { return __uint_as_float(u << 16); }
__device__ __forceinline__ float bf_hi(unsigned u) { return __uint_as_float(u & 0xffff0000u); }
__device__ __forceinline__ float rbf(float f) { return bf_lo(pk2(f, f) & 0xffffu); }
__device__ __forceinline__ void glds16(gcptr sbase, unsigned voff, unsigned lds_base) {
    asm volatile("s_mov_b32 m0, %2\n\ts_nop 0\n\tglobal_load_lds_dwordx4 %0, %1" :: "v"(voff), "s"(sbase), "s"(lds_base) : "memory");
}
__device__ __forceinline__ void glds16_dev(gcptr sbase, unsigned voff, unsigned lds_base) {
    asm volatile("s_mov_b32 m0, %2\n\ts_nop 0\n\tglobal_load_lds_dwordx4 %0, %1 sc1" :: "v"(voff), "s"(sbase), "s"(lds_base) : "memory");
}
__device__ __forceinline__ void touch(gcptr sbase, unsigned voff, unsigned lds_dump) {
    asm volatile("s_mov_b32 m0, %2\n\ts_nop 0\n\tglobal_load_lds_dword %0, %1" :: "v"(voff), "s"(sbase), "s"(lds_dump) : "memory");
}
__device__ __forceinline__ void gload16_dev(u32x4& d, gcptr sbase, unsigned voff) {
    asm volatile("global_load_dwordx4 %0, %1, %2 sc1" : "=v"(d) : "v"(voff), "s"(sbase) : "memory");
}
__device__ __forceinline__ void gload16(u32x4& d, gcptr sbase, unsigned voff) {
    asm volatile("global_load_dwordx4 %0, %1, %2" : "=v"(d) : "v"(voff), "s"(sbase) : "memory");
}
__device__ __forceinline__ void gload8_dev(u64& d, gcptr sbase, unsigned voff) {
    asm volatile("global_load_dwordx2 %0, %1, %2 sc1" : "=v"(d) : "v"(voff), "s"(sbase) : "memory");
}
__device__ __forceinline__ void gstore16_dev(gptr sbase, unsigned voff, u32x4 d) {
    asm volatile("global_store_dwordx4 %0, %1, %2 sc1\n\ts_nop 1" :: "v"(voff), "v"(d), "s"(sbase) : "memory");
}
__device__ __forceinline__ s16x4 trd(lptr p) { return __builtin_amdgcn_ds_read_tr16_b64_v4i16((LAS s16x4*)p); }
__device__ __forceinline__ bf16x8 cat4(s16x4 a, s16x4 b) { return __builtin_shufflevector(a, b, 0, 1, 2, 3, 4, 5, 6, 7); }
__device__ __forceinline__ u32x4 pack8(const f32x16& a, int o, float s) {
    u32x4 r;
    r[0] = pk2(a[o + 0] * s, a[o + 1] * s); r[1] = pk2(a[o + 2] * s, a[o + 3] * s);
    r[2] = pk2(a[o + 4] * s, a[o + 5] * s); r[3] = pk2(a[o + 6] * s, a[o + 7] * s);
    return r;
}
__device__ __forceinline__ u32x4 pack8n(const f32x16& a, int o) {
    u32x4 r;
    r[0] = pk2(a[o + 0], a[o + 1]); r[1] = pk2(a[o + 2], a[o + 3]);
    r[2] = pk2(a[o + 4], a[o + 5]); r[3] = pk2(a[o + 6], a[o + 7]);
    return r;
}
__device__ __forceinline__ float half_swap_add(float v) {
    auto r = __builtin_amdgcn_permlane32_swap(__float_as_uint(v), __float_as_uint(v), false, false);
    return __uint_as_float(r[0]) + __uint_as_float(r[1]);
}
__device__ __forceinline__ float half_swap_max(float v) {
    auto r = __builtin_amdgcn_permlane32_swap(__float_as_uint(v), __float_as_uint(v), false, false);
    return __builtin_fmaxf(__uint_as_float(r[0]), __uint_as_float(r[1]));
}
__device__ __forceinline__ float max16(const f32x16& s) {
    float a = __builtin_fmaxf(__builtin_fmaxf(s[0], s[1]), s[2]), b = __builtin_fmaxf(__builtin_fmaxf(s[3], s[4]), s[5]);
    a = __builtin_fmaxf(__builtin_fmaxf(a, s[6]), s[7]);   b = __builtin_fmaxf(__builtin_fmaxf(b, s[8]), s[9]);
    a = __builtin_fmaxf(__builtin_fmaxf(a, s[10]), s[11]); b = __builtin_fmaxf(__builtin_fmaxf(b, s[12]), s[13]);
    a = __builtin_fmaxf(__builtin_fmaxf(a, s[14]), s[15]);
    return __builtin_fmaxf(a, b);
}
__device__ __forceinline__ lptr lbase(lptr p) { unsigned u = (unsigned)(uintptr_t)p; asm volatile("" : "+v"(u)); return (lptr)(uintptr_t)u; }
__device__ __forceinline__ unsigned split_hl(float v) {
    const float hi = rbf(v);
    return pk2(hi, v - hi);
}
__device__ __forceinline__ void flag_load_w0(u64& d, gcptr sbase, unsigned voff, int w) {
    asm volatile("s_cmp_lg_u32 %3, 0\n\ts_cbranch_scc1 1f\n\tglobal_load_dwordx2 %0, %1, %2 sc1\n1:" : "+v"(d) : "v"(voff), "s"(sbase), "s"(w) : "memory", "scc");
}
__device__ __forceinline__ bool flags_ok(u64 v, u64 epoch) {
    u64 m; asm volatile("v_cmp_eq_u64 %0, %1, %2" : "=s"(m) : "s"(epoch), "v"(v)); return m == ~0ull;
}
__device__ __forceinline__ void poll_flags(gcptr base, unsigned voff, u64 epoch) {
#pragma nounroll
    for (unsigned spins = 0; spins < (1u << 20); ++spins) {
        const u64 v = __hip_atomic_load((GAS const u64*)(base + voff), RLX_AGENT);
        if (__all(v == epoch)) break;
        __builtin_amdgcn_s_sleep(2);
    }
    asm volatile("" ::: "memory");
}

__device__ constexpr int b_pos(int n) {
    constexpr int t[36] = {16, 17, 18, 0, 1, 2, 3, 4, 5, 16, 17, 18, 0, 1, 2, 3, 6, 7, 4, 5, 8, 9, 0, 1, 2, 3, 10, 11, 16, 17, 18, 6, 4, 5, 12, 13};
    return t[n];
}
__device__ constexpr int u_pair(int u) {
    constexpr int t[28] = {7, 1, 8, 0, 2, 3, 6, 4, 5, 7, 8, 1, 0, 2, 3, 4, 6, 5, 7, 8, 0, 1, 2, 3, 4, 5, 6, 7};
    return t[u];
}
template <int V> struct IC { static constexpr int value = V; };
template <int I, int N, class F> __device__ __forceinline__ void static_for(F&& f) {
    if constexpr (I < N) { f(IC<I>{}); static_for<I + 1, N>(f); }
}
#define SFOR(var, N) static_for<0, (N)>([&](auto ic_##var) __attribute__((always_inline)) { constexpr int var = decltype(ic_##var)::value;
#define SEND });

__device__ __forceinline__ void attn_slow(const __amdgpu_buffer_rsrc_t rs_ws, int head, bf16x8 q, int lane, f32x16& o) {
    float m = -__builtin_inff();
    o = f32x16{};
    const bool data = (lane & 16) == 0;
    const u32x4 ones = {0x3f803f80u, 0x3f803f80u, 0x3f803f80u, 0x3f803f80u};
    const unsigned vl = (unsigned)((lane >> 5) * 256 + (lane & 15) * 16);
#pragma nounroll
    for (int t = 0; t < 64; ++t) {
        const u32x4 k = __builtin_amdgcn_raw_buffer_load_b128(rs_ws, lane * 16, WS_K + head * 65536 + t * 1024, 16);
        u32x4 v0 = __builtin_amdgcn_raw_buffer_load_b128(rs_ws, vl, WS_V + head * 65536 + t * 1024, 16);
        u32x4 v1 = __builtin_amdgcn_raw_buffer_load_b128(rs_ws, vl + 512, WS_V + head * 65536 + t * 1024, 16);
        if (!data) { v0 = ones; v1 = ones; }
        f32x16 s = MFMA32(PB(k), q, f32x16{});
        const float mn = __builtin_fmaxf(m, half_swap_max(max16(s)));
        const float al = __builtin_amdgcn_exp2f(m - mn);
        m = mn;
#pragma unroll
        for (int r = 0; r < 16; ++r) { s[r] = __builtin_amdgcn_exp2f(s[r] - mn); o[r] *= al; }
        o = MFMA32(PB(v0), PB(pack8n(s, 0)), o);
        o = MFMA32(PB(v1), PB(pack8n(s, 8)), o);
    }
}

extern "C" __global__ __launch_bounds__(512, 2) void fused_block(const char* __restrict__ x_, const char* __restrict__ wq_, const char* __restrict__ wo_,
        const char* __restrict__ wg_, const char* __restrict__ wu_, const char* __restrict__ wd_, char* ws_, char* __restrict__ out_) {
    __shared__ __attribute__((aligned(16))) char smem_[163840];
    const int lane = threadIdx.x & 63;
    const int w = __builtin_amdgcn_readfirstlane(threadIdx.x >> 6);
    const int xcd = blockIdx.x & 7, i = blockIdx.x >> 3, G = w >> 2, tau = w & 3;
    const lptr smem = (lptr)smem_;
    const unsigned lds0 = (unsigned)(uintptr_t)smem, lin = lane * 16;
    gptr ws = (gptr)ws_;
    if (w == 7) {
        u64 pc; unsigned len;
        asm volatile("s_getpc_b64 %0\n.Lpc_ref:\n\ts_mov_b32 %1, .Lcode_end-.Lpc_ref-4" : "=s"(pc), "=s"(len));
        const unsigned o = (unsigned)(i * 64 + lane) * 128;
        touch((gcptr)pc, o < len ? o : len, lds0 + L_DUMP);
    }
    u64 ep;
    gload8_dev(ep, (gcptr)ws + WS_N, 0u);
    const int rbl = i >> 2, jl = i & 3, cq = xcd & 3;
    const int rb = (xcd >> 2) * 8 + rbl, tt = rb * 4 + tau, rot = (rbl + 2 * jl) & 7;
    const bool tyb = jl == 3;
    const unsigned vx0 = (unsigned)((lane >> 3) * 1024 + (((lane & 7) ^ (lane >> 4)) * 16));
    const unsigned vx1 = (unsigned)((8 + (lane >> 3)) * 1024 + (((lane & 7) ^ (4 + (lane >> 4))) * 16));
    gcptr xp = (gcptr)x_ + (size_t)(tt * 32 + G * 16) * 1024;
    const unsigned xd = lds0 + tau * 4096 + G * 2048, wd0 = lds0 + w * 1024;
    const unsigned sw0 = (unsigned)(((((lane >> 2) ^ (lane >> 4)) & 1) * 64) + (lane & 3) * 16), sw1 = sw0 ^ 32u;
    const unsigned vq = (lane >> 3) * 3072 + sw0;
    gcptr s1p = (gcptr)wq_ + (4 * cq + jl) * 128 + w * 8 * 3072;
    const int p2 = tyb ? 3072 : 3584;
    const unsigned v2a = (lane >> 3) * p2 + sw0, v2b = (lane >> 3) * p2 + sw1, v3a = (lane >> 3) * 3584 + sw0, v3b = (lane >> 3) * 3584 + sw1;
    const int fu2 = 7 * cq + 2 * jl, fu3 = tyb ? fu2 : fu2 + 1;
    gcptr g2a = (tyb ? (gcptr)wq_ + (16 + 2 * cq) * 128 : (gcptr)wg_ + fu2 * 128) + w * 8 * p2;
    gcptr g2b = (tyb ? (gcptr)wq_ + (17 + 2 * cq) * 128 : (gcptr)wu_ + fu2 * 128) + w * 8 * p2;
    gcptr s3a = (gcptr)wg_ + fu3 * 128 + w * 8 * 3584, s3b = (gcptr)wu_ + fu3 * 128 + w * 8 * 3584;
#define RS(D) (((D) + rot) & 7)
    auto u_ld = [&](auto ic) __attribute__((always_inline)) {
        constexpr int e = decltype(ic)::value, u = e >> 1, hf_ = e & 1, j = u % 5, D = (u / 5) * 2 + (j >= 3), o = u_pair(u) * 16384;
        if constexpr (u >= 20) { if constexpr (hf_) glds16(s3b + (size_t)RS(u - 20) * 64 * 3584, v3b, wd0 + o + 8192); else glds16(s3a + (size_t)RS(u - 20) * 64 * 3584, v3a, wd0 + o); }
        else if constexpr (j == 0 || j == 3) { if constexpr (hf_) glds16(xp + RS(D) * 128, vx1, xd + o + 1024); else glds16(xp + RS(D) * 128, vx0, xd + o); }
        else if constexpr (j == 1) glds16(s1p + (size_t)RS(D + hf_) * 64 * 3072, vq, wd0 + o + hf_ * 8192);
        else { if constexpr (hf_) glds16(g2b + (size_t)RS(D) * 64 * p2, v2b, wd0 + o + 8192); else glds16(g2a + (size_t)RS(D) * 64 * p2, v2a, wd0 + o); }
    };
    SFOR(e, 6) u_ld(IC<e>{}); SEND
    const __amdgpu_buffer_rsrc_t rs_ws = __builtin_amdgcn_make_buffer_rsrc((void*)ws_, 0, (int)WS_BYTES, 0x00020000);
    if (w < 2) {
        const unsigned o = (lane & 1) ? 0u : 0x3f803f80u;
        *(LAS u32x4*)(smem + L_ONES + w * 1024 + lin) = u32x4{o, o, o, o};
    } else if (w == 2) *(LAS unsigned*)(smem + L_CNT + lane * 4) = 0u;
    const int l15 = lane & 15, dq = l15 >> 2, kr = tau + 2 * G;
    u32x4 xr[32];
    lptr wb[4][2];
#pragma unroll
    for (int q = 0; q < 4; ++q)
        wb[q][0] = lbase(smem + (((q + kr) & 3) * 16 + (lane >> 5) * 8 + dq) * 128 + ((G ^ (dq >> 1)) & 1) * 64 + ((lane >> 4) & 1) * (8192 + 32) + (lane & 3) * 8);
    unsigned xb0 = lds0 + tau * 4096 + (lane & 31) * 128 + (((lane >> 5) ^ ((lane >> 1) & 7)) * 16), ng = ((lane >> 4) & 1) * 8192;
    PIN(xb0); PIN(ng);
    const unsigned selv = l15 == ((lane >> 4) & 1) ? 0x3f803f80u : 0u;
    const bf16x8 self = PB((u32x4{selv, selv, selv, selv}));
    float s2 = 0.f, s2b = 0.f;
#define X_STAT(d) do { const unsigned u_ = xr[((d) >> 3) * 4 + (((d) >> 2) & 1)][(d) & 3]; const float lo_ = bf_lo(u_), hi_ = bf_hi(u_); \
        s2 = __builtin_fmaf(lo_, lo_, s2); PIN(s2); s2b = __builtin_fmaf(hi_, hi_, s2b); PIN(s2b); } while (0)
    constexpr int WD = 4;
    bf16x8 wf[6];
    f32x16 aa[3] = {}, a3[2] = {};
    f32x4 cs[3] = {}, sm = {};
    const lptr csw = smem + L_CS + G * 1536 + lin + tau * 4;
    const lptr csr = smem + L_CS + G * 1536 + (lane & 31) * 16;
#define CS_WRITE(m) do { if (lane < 16) { *(LAS unsigned*)(csw + (m) * 512) = split_hl(cs[m][0]); *(LAS unsigned*)(csw + (m) * 512 + 256) = split_hl(cs[m][1]); } cs[m] = f32x4{}; } while (0)
#define LP(u) lbase((lptr)(uintptr_t)(u))
#define LU(p) ((unsigned)(uintptr_t)(p))
    auto a_frag = [&](auto Dc, auto nc) __attribute__((always_inline)) {
        constexpr int D = decltype(Dc)::value, n = decltype(nc)::value, q = n / 3, t = n % 3;
        constexpr unsigned off = t == 0 ? u_pair((D >> 1) * 5 + 1) * 16384 + (D & 1) * 8192 : u_pair((D >> 1) * 5 + 2 + (D & 1) * 2) * 16384;
        constexpr unsigned hi = off & ~65535u, lo = off & 65535u;
        const lptr P = (lptr)(uintptr_t)(t == 0 ? (LU(wb[q][0]) ^ ng) + hi : t == 2 ? LU(wb[q][0]) ^ (32u | hi) : LU(wb[q][0]) ^ hi) + lo;
        const bf16x8 f = cat4(trd(P), trd(P + 512));
        wb[q][0] = lbase(wb[q][0]);
        return f;
    };
    auto x_frag = [&](auto Dc, auto qc) __attribute__((always_inline)) {
        constexpr int D = decltype(Dc)::value, q = decltype(qc)::value;
        constexpr unsigned off = u_pair((D >> 1) * 5 + (D & 1) * 3) * 16384;
        unsigned sx = (unsigned)(((q + kr) & 3) * 32) | (off & ~65535u);
        asm volatile("" : "+s"(sx));
        xr[4 * D + q] = *(LAS const u32x4*)((lptr)(uintptr_t)(xb0 ^ sx) + (off & 65535u));
    };
    SFOR(e, 4) u_ld(IC<6 + e>{}); SEND
    WAITV(4); BAR();
    x_frag(IC<0>{}, IC<0>{});
    SFOR(m, WD) wf[m] = a_frag(IC<0>{}, IC<m>{}); SEND
    x_frag(IC<0>{}, IC<1>{});
    SFOR(D, 8)
        constexpr int un = 9 + ((D - 1) >> 1) * 5 + ((D - 1) & 1) * 2;
        SFOR(n, 12)
            if constexpr (n == 12 - WD && D < 7) {
                if constexpr (D == 0) WAITV(8); else WAITV(6);
                BAR();
                x_frag(IC<(D + 1) & 7>{}, IC<0>{});
            }
            if constexpr (n == 0 && D > 0) BAR();
            aa[n % 3] = MFMA32(wf[n % WD], PB(xr[4 * D + n / 3]), aa[n % 3]); PIN(aa[n % 3]);
            if constexpr (n / 3 == 1) { cs[n % 3] = MFMA16(self, wf[n % WD], cs[n % 3]); PIN(cs[n % 3]); }
            if constexpr (n + WD < 12) wf[n % WD] = a_frag(IC<D>{}, IC<(n + WD) % 12>{});
            else if constexpr (D < 7) wf[n % WD] = a_frag(IC<(D + 1) & 7>{}, IC<(n + WD) % 12>{});
            if constexpr (n != 3 && n != 4 && n != 5 && n < 11) X_STAT(8 * D + (n < 3 ? n : n - 3));
            if constexpr (n == 1 || n == 6) { sm = MFMA16(self, PB(xr[4 * D + n / 6]), sm); PIN(sm); }
            if constexpr (D == 7 && n == 11) *(LAS float*)(smem + L_STAT + w * 256 + (lane & 31) * 8 + 4) = half_swap_add(s2 + s2b);
            if constexpr (n == 2) x_frag(IC<D>{}, IC<2>{});
            if constexpr (n == 5) x_frag(IC<D>{}, IC<3>{});
            if constexpr (n == 10 && D < 7) x_frag(IC<(D + 1) & 7>{}, IC<1>{});
            if constexpr (D == 0 && n < 8) u_ld(IC<10 + (n & 7)>{});
            if constexpr (D > 0 && (n & 1) == 0 && n < ((D & 1) ? 8 : 12)) u_ld(IC<(D > 0 ? 2 * un + n / 2 : 0)>{});
            if constexpr (D == 7 && n >= 7 && n < 10) CS_WRITE(n - 7);
            if constexpr (D == 7 && n == 10) {
                if (lane < 16) { *(LAS float*)(smem + L_STAT + w * 256 + lane * 8) = sm[0]; *(LAS float*)(smem + L_STAT + w * 256 + 128 + lane * 8) = sm[1]; }
            }
            SB();
        SEND
    SEND
#pragma unroll
    for (int q = 0; q < 4; ++q) wb[q][1] = LP(LU(wb[q][0]) ^ 32u);
    asm volatile("s_waitcnt vmcnt(4) lgkmcnt(0)\n\ts_barrier" ::: "memory");
    PIN(ep);
    const u64 epoch = (((u64)(unsigned)__builtin_amdgcn_readfirstlane((unsigned)(ep >> 32)) << 32) | (unsigned)__builtin_amdgcn_readfirstlane((unsigned)ep)) + 0x9E3779B97F4A7C15ull;
    float sd, rs, ns;
    u32x4 mu4;
    const int u1 = 4 * cq + jl;
    {
        const f32x2 sta = *(LAS const f32x2*)(smem + L_STAT + tau * 256 + (lane & 31) * 8), stb = *(LAS const f32x2*)(smem + L_STAT + 1024 + tau * 256 + (lane & 31) * 8);
        const bf16x8 c0 = *(LAS const bf16x8*)(csr), c1 = *(LAS const bf16x8*)(csr + 512), c2 = *(LAS const bf16x8*)(csr + 1024);
        const float mean = (sta[0] + stb[0]) * (1.f / 512.f);
        const float var = __builtin_fmaxf((sta[1] + stb[1]) * (1.f / 512.f) - mean * mean, 0.f);
        const unsigned nm = lane < 32 ? pk2(-mean, -mean) : 0u;
        mu4 = u32x4{nm, nm, nm, nm};
        aa[0] = MFMA32(c0, PB(mu4), aa[0]); aa[1] = MFMA32(c1, PB(mu4), aa[1]); aa[2] = MFMA32(c2, PB(mu4), aa[2]);
        PIN(aa[0]); PIN(aa[1]); PIN(aa[2]);
        sd = rbf(__builtin_amdgcn_sqrtf(rbf(rbf(var) + 1e-5f)));
        rs = __builtin_amdgcn_rcpf(sd);
        ns = rs * -1.4426950408889634f;
        const float sc1 = u1 < 8 ? rs * (0.25f * 1.4426950408889634f) : rs;
        const unsigned o1 = WS_Q + (unsigned)((u1 * 4 + 2 * G) * 64 + tt) * 1024;
        gstore16_dev(ws + o1, lin, pack8(aa[0], 0, sc1));
        gstore16_dev(ws + (o1 + 65536), lin, pack8(aa[0], 8, sc1));
    }
    constexpr int WB = 4;
#define S_FRAG(n) cat4(trd(wb[((n) >> 1) & 3][(n) & 1] + (((n) >> 3) & 3) * 16384), trd(wb[((n) >> 1) & 3][(n) & 1] + (((n) >> 3) & 3) * 16384 + 512))
#define REBASE(d) do { _Pragma("unroll") for (int q = 0; q < 4; ++q) { wb[q][0] = lbase(wb[q][0] + (d)); wb[q][1] = lbase(wb[q][1] + (d)); } } while (0)
#pragma unroll
    for (int m = 0; m < WB; ++m) wf[m] = S_FRAG(m);
    const lptr tw = smem + L_COMB + w * 2048 + ((lane & 31) >> 2) * 128 + (lane >> 5) * 64 + (lane & 3) * 16;
    const lptr tr = smem + L_COMB + w * 2048 + ((lane >> 4) & 1) * 512 + (lane >> 5) * 128 + ((lane >> 1) & 1) * 64 + dq * 16 + (lane & 1) * 8;
    const unsigned vto = (unsigned)(((lane >> 4) & 1) * 512 + (lane >> 5) * 256 + l15 * 16);
    const unsigned ov = WS_V + (unsigned)((cq * 8 + 2 * G) * 64 + tt) * 1024;
    u32x4 hq0 = {0u, 0u, 0u, 0u}, hq1 = {0u, 0u, 0u, 0u};
    const int head = xcd * 4 + (i >> 3), qblk = i & 7;
    gcptr kvsrc = (gcptr)ws + (G ? WS_V : WS_K) + (size_t)head * 65536 + ((w >> 1) & 1) * 32768;
    const int kv0 = qblk * 4 + (w & 1) + 1 - G;
#define KV_LOAD(ch) glds16_dev(kvsrc + ((kv0 + 2 * (ch)) & 31) * 1024, lin, lds0 + (ch) * 8192 + w * 1024)
    const unsigned f1o = (unsigned)((((lane < 16 ? 8 : lane < 32 ? 16 : 0) + (head >> 2)) * 16 + l15) * 8);
    u64 fl = 0;
    bool kvin = false, kvgo = false;
    LAS unsigned* kvw = (LAS unsigned*)(smem + L_CNT + 8);
    unsigned kvs = 0;
    float ht = 0.f;
#define H_ELT(A, e) A[e] = A[8 + (e)] * __builtin_amdgcn_rcpf(__builtin_fmaf(__builtin_amdgcn_exp2f(A[e] * ns), sd, sd))
    const unsigned oh2 = WS_H + (unsigned)(tt * 112 + fu2 * 4 + G * 2) * 1024;
    gcptr qp = (gcptr)ws + WS_Q + (size_t)(head * 64 + qblk * 8 + w) * 1024;
    gcptr kp0 = (gcptr)ws + WS_K + (size_t)head * 65536 + ((w & 2) ? 0 : (w & 1) * 32768 + qblk * 4096);
#define QKV_FIRST() do { glds16_dev(qp, lin, wd0 + L_COMB); glds16_dev(kp0, lin, wd0 + L_COMB + 8192); KV_LOAD(0); KV_LOAD(1); KV_LOAD(2); KV_LOAD(3); } while (0)
    SFOR(n, 64)
        if constexpr (n == 20) {
            WAITV(0); BAR();
            if (threadIdx.x == 0) {
                __hip_atomic_store((gu64*)(ws + WS_F1 + (u1 * 16 + rb) * 8), epoch, RLX_AGENT);
                if (tyb) { __hip_atomic_store((gu64*)(ws + WS_F1 + ((16 + 2 * cq) * 16 + rb) * 8), epoch, RLX_AGENT); __hip_atomic_store((gu64*)(ws + WS_F1 + ((17 + 2 * cq) * 16 + rb) * 8), epoch, RLX_AGENT); }
            }
            if (!tyb) { gstore16_dev(ws + oh2, lin, hq0); gstore16_dev(ws + (oh2 + 1024), lin, hq1); }
        }
        if constexpr (n < 20 && n % 5 == 0) __builtin_amdgcn_s_setprio(3 - n / 5);
        if constexpr (n >= 20 && (n - 20) % 11 == 0) __builtin_amdgcn_s_setprio(3 - (n - 20) / 11);
        if constexpr (n == 32 - WB) REBASE(65536);
        if constexpr (n == 36) flag_load_w0(fl, (gcptr)ws + WS_F1, f1o, w);
        if constexpr (n == 45 || n == 54) {
            if (w == 0 && !kvin) { WAITV(0); if (flags_ok(fl, epoch)) __hip_atomic_store(kvw, 1u, RLX_WG); }
            flag_load_w0(fl, (gcptr)ws + WS_F1, f1o, w);
            kvs = __hip_atomic_load(kvw, RLX_WG);
        }
        if constexpr (n == 48 || n == 57) { kvgo = !kvin && __builtin_amdgcn_readfirstlane(kvs); kvin = kvin || kvgo; if (kvgo) { glds16_dev(qp, lin, wd0 + L_COMB); glds16_dev(kp0, lin, wd0 + L_COMB + 8192); } }
        if constexpr (n == 50 || n == 59) { if (kvgo) { KV_LOAD(0); KV_LOAD(1); } }
        if constexpr (n == 52 || n == 61) { if (kvgo) { KV_LOAD(2); KV_LOAD(3); } }
        a3[n & 1] = MFMA32(wf[n % WB], PB(xr[n >> 1]), a3[n & 1]); PIN(a3[n & 1]);
        if constexpr ((n & 6) == 2) { cs[n & 1] = MFMA16(self, wf[n % WB], cs[n & 1]); PIN(cs[n & 1]); }
        if constexpr (n + WB < 64) wf[n % WB] = S_FRAG(n + WB);
        if constexpr (n >= 1 && n < 12 && n % 3 == 1) u_ld(IC<52 + ((n / 3) & 3)>{});
        if constexpr (n < 19) {
            if (tyb) {
                if constexpr (n == 0 || n == 4) *(LAS u32x4*)tw = pack8(aa[1 + n / 4], 0, rs);
                if constexpr (n == 1 || n == 5) *(LAS u32x4*)(tw + 1024) = pack8(aa[1 + n / 4], 8, rs);
                if constexpr (n == 2 || n == 6) { hq0 = __builtin_bit_cast(u32x4, cat4(trd(tr), trd(tr + 256))); hq1 = __builtin_bit_cast(u32x4, cat4(trd(tr + 1024), trd(tr + 1280))); }
                if constexpr (n == 5 || n == 9) { gstore16_dev(ws + (ov + (n / 9) * 65536), vto, hq0); gstore16_dev(ws + (ov + (4 + n / 9) * 65536), vto, hq1); }
            } else {
                if constexpr (n > 0 && n < 17) { constexpr int t = 1 + (((n - 1) >> 3) & 1), e = (n - 1) & 7; aa[t][e] = aa[t][8 + e] * __builtin_amdgcn_rcpf(__builtin_fmaf(ht, sd, sd)); PIN(aa[t]); }
                if constexpr (n < 16) { ht = __builtin_amdgcn_exp2f(aa[1 + ((n >> 3) & 1)][n & 7] * ns); PIN(ht); }
                if constexpr (n == 17) { hq0 = pack8n(aa[1], 0); PIN(hq0); }
                if constexpr (n == 18) { hq1 = pack8n(aa[2], 0); PIN(hq1); }
            }
        }
        if constexpr (n == 60) CS_WRITE(0);
        if constexpr (n == 61) CS_WRITE(1);
        SB();
    SEND
#undef S_FRAG
#undef REBASE
    if (w == 0 && !kvin) { WAITV(0); if (flags_ok(fl, epoch)) __hip_atomic_store(kvw, 1u, RLX_WG); }
    asm volatile("s_waitcnt lgkmcnt(0)\n\ts_barrier" ::: "memory");
    if (!kvin && __builtin_amdgcn_readfirstlane(__hip_atomic_load(kvw, RLX_WG))) { kvin = true; QKV_FIRST(); }
    a3[0] = MFMA32(*(LAS const bf16x8*)(csr), PB(mu4), a3[0]); a3[1] = MFMA32(*(LAS const bf16x8*)(csr + 512), PB(mu4), a3[1]);
#pragma unroll
    for (int e = 0; e < 8; ++e) { H_ELT(a3[0], e); H_ELT(a3[1], e); }
    u32x4 hp0 = pack8n(a3[0], 0), hp1 = pack8n(a3[1], 0);
    PIN(hp0); PIN(hp1);
#undef H_ELT
    if (!kvin) {
        if (w == 0) { poll_flags((gcptr)ws + WS_F1, f1o, epoch); __hip_atomic_store(kvw, 1u, RLX_WG); }
#pragma nounroll
        for (unsigned spins = 0; spins < (1u << 24) && !__builtin_amdgcn_readfirstlane(__hip_atomic_load(kvw, RLX_WG)); ++spins) __builtin_amdgcn_s_sleep(1);
        QKV_FIRST();
    }
#undef QKV_FIRST
    WAITV(0); BAR();
    PIN(fl);
    gstore16_dev(ws + (WS_H + (unsigned)(tt * 112 + fu3 * 4 + G * 2) * 1024), lin, hp0);
    gstore16_dev(ws + (WS_H + (unsigned)(tt * 112 + fu3 * 4 + G * 2 + 1) * 1024), lin, hp1);
    int lane2 = (int)(lin >> 4);
    PIN(lane2);
    const int l15b = lane2 & 15;
    const int tb = xcd * 4 + (i >> 3), cb = i & 7;
    const int tt4 = tb * 2 + (w & 1), kq = w >> 1;
    const bf16x8 qa = *(LAS const bf16x8*)(smem + L_COMB + tau * 2048 + lin), qb = *(LAS const bf16x8*)(smem + L_COMB + tau * 2048 + 1024 + lin);
    const bf16x8 k0_ = *(LAS const bf16x8*)(smem + L_COMB + 8192 + 2048 + lin);
    bf16x8 kc = *(LAS const bf16x8*)(smem + L_COMB + 8192 + G * 1024 + lin);
    f32x16 ca, cb_, sa, sb;
    {
        const f32x16 s = MFMA32(k0_, qa, f32x16{});
        const f32x16 s2 = MFMA32(k0_, qb, f32x16{});
        const float na = -half_swap_max(max16(s)), nb = -half_swap_max(max16(s2));
#pragma unroll
        for (int r = 0; r < 16; ++r) { ca[r] = na; cb_[r] = nb; }
        sa = MFMA32(kc, qa, ca);
    }
    const bool data = (lane2 & 16) == 0;
    const unsigned vdat = 4096 + G * 2048 + (lane2 >> 5) * 256 + l15b * 16;
    const unsigned vone = L_ONES + (((l15b + 12) & 8) ? 0 : 64) + ((l15b & 11) ? 16 : 0);
    const unsigned vinc = data ? 8192u : 0u;
    f32x16 oa = {}, ob = {};
    u32x4 p0 = {0u, 0u, 0u, 0u}, p1 = {0u, 0u, 0u, 0u};
    bf16x8 v0, v1 = {0, 0, 0, 0, 0, 0, 0, 0};
    const unsigned bvo = (lane2 >> 3) * 1024 + ((((lane2 >> 2) ^ (lane2 >> 4)) & 1) * 64) + (lane2 & 3) * 16;
    gcptr bd = (gcptr)wd_ + w * 8192 + cb * 128, bo = (gcptr)wo_ + w * 8192 + cb * 128;
#define B_POS(n) b_pos(n)
#define B_LOAD(n) glds16((n) < 28 ? bd + (n) * 65536 : bo + ((((n) - 28) + cb) & 7) * 65536, bvo, lds0 + B_POS(n) * 8192 + w * 1024)
#define EXPR(S, a_, b_) do { _Pragma("unroll") for (int r = (a_); r < (b_); ++r) S[r] = __builtin_amdgcn_exp2f(S[r]); } while (0)
#define HALF(SC, MF_A, MF_B, MF_C, LD_A, LD_B, HOOK0, HOOK) do { \
        MF_A; SB(); LD_A; EXPR(SC, 0, 5); PIN(SC); SB(); \
        MF_B; SB(); LD_B; EXPR(SC, 5, 8); PIN(SC); SB(); HOOK0; SB(); p0 = pack8n(SC, 0); PIN(p0); SB(); EXPR(SC, 8, 10); PIN(SC); SB(); \
        MF_C; SB(); EXPR(SC, 10, 14); PIN(SC); SB(); HOOK; SB(); EXPR(SC, 14, 16); PIN(SC); p1 = pack8n(SC, 8); PIN(p1); SB(); \
    } while (0)
    lptr kp = lbase(smem + G * 2048 + lin);
    lptr vp = lbase(smem + (data ? vdat : vone));
    auto segment = [&](auto hook) __attribute__((always_inline)) {
        SFOR(t, 4)
            constexpr int ko = (t >> 1) * 8192 + (t & 1) * 1024, vo = (t & 1) * 1024;
            __builtin_amdgcn_s_setprio(3 - t);
            HALF(sa, do { sb = MFMA32(kc, qb, cb_); PIN(sb); } while (0), do { ob = MFMA32(v1, PB(p1), ob); PIN(ob); } while (0), do { oa = MFMA32(v0, PB(p0), oa); PIN(oa); } while (0),
                 do { kc = *(LAS const bf16x8*)(kp + ko); v0 = *(LAS const bf16x8*)(vp + vo); } while (0), v1 = *(LAS const bf16x8*)(vp + vo + 512), hook(IC<2 * t + 8>{}), hook(IC<2 * t>{}));
            HALF(sb, do { sa = MFMA32(kc, qa, ca); PIN(sa); } while (0), do { oa = MFMA32(v1, PB(p1), oa); PIN(oa); } while (0), do { ob = MFMA32(v0, PB(p0), ob); PIN(ob); } while (0),
                 (void)0, (void)0, hook(IC<2 * t + 9>{}), hook(IC<2 * t + 1>{}));
            if constexpr ((t & 1) == 1) vp = lbase(vp + vinc);
        SEND
        kp = lbase(kp + 16384);
    };
#define HOOK(u) [&](auto ic_) __attribute__((always_inline)) { constexpr int u = decltype(ic_)::value;
#define ZBAR(n) asm volatile("s_waitcnt vmcnt(" #n ") lgkmcnt(0)\n\ts_barrier" ::: "memory")
    const unsigned f2o = (unsigned)(((tb >> 1) * 16 + l15b) * 8);
    gcptr hsrc = (gcptr)ws + WS_H + (size_t)(tt4 * 112 + kq) * 1024;
    u32x4 hf[28];
#define H_LOAD(n) gload16_dev(hf[n], hsrc + (n) * 4096, lin)
    segment(HOOK(u)
        if constexpr (u < 4) { KV_LOAD(4 + 3 * u); KV_LOAD(5 + 3 * u); KV_LOAD(6 + 3 * u); }
    });
#undef KV_LOAD
    ZBAR(0);
    if (threadIdx.x == 0) __hip_atomic_store((gu64*)(ws + WS_F2 + (rb * 16 + cq * 4 + jl) * 8), epoch, RLX_AGENT);
    segment(HOOK(u)
        if constexpr (u < 5) B_LOAD(u);
        if constexpr (u == 6) flag_load_w0(fl, (gcptr)ws + WS_F2, f2o, w);
    });
    if (w == 0) { WAITV(0); if (!flags_ok(fl, epoch)) poll_flags((gcptr)ws + WS_F2, f2o, epoch); }
    ZBAR(63);
    segment(HOOK(u)
        if constexpr (u < 3) { H_LOAD(3 * u); H_LOAD(3 * u + 1); H_LOAD(3 * u + 2); }
        if constexpr (u == 3) { B_LOAD(5); B_LOAD(6); }
    });
    PIN(lane2);
    const int dqb = (lane2 & 15) >> 2;
    lptr db[2][3];
#pragma unroll
    for (int t = 0; t < 2; ++t) {
        db[t][0] = lbase(smem + (kq * 16 + (lane2 >> 5) * 4 + dqb) * 128 + (((dqb >> 1) & 1) ^ t) * 64 + ((lane2 >> 4) & 1) * 32 + (lane2 & 3) * 8);
        db[t][1] = lbase(db[t][0] + 65536); db[t][2] = lbase(db[t][0] + 131072);
    }
    f32x16 dd[2] = {};
#define D_FRAG(t, n) cat4(trd(db[t][B_POS(n) >> 3] + (B_POS(n) & 7) * 8192), trd(db[t][B_POS(n) >> 3] + (B_POS(n) & 7) * 8192 + 1024))
#define D_PRE(n) do { wf[0] = D_FRAG(0, n); wf[1] = D_FRAG(1, n); } while (0)
#define D_UNIT(u, b0, b1) do { constexpr int t_ = (u) < 8, n_ = (b0) + ((u) & 7); if constexpr (n_ < (b1)) { \
        PIN(hf[n_]); \
        dd[t_] = MFMA32(wf[t_], PB(hf[n_]), dd[t_]); PIN(dd[t_]); \
        if constexpr (n_ + 1 < (b1)) wf[t_] = D_FRAG(t_, n_ + 1); } } while (0)
    ZBAR(0);
    D_PRE(0);
    segment(HOOK(u)
        D_UNIT(u, 0, 7);
        if constexpr (u == 0) { B_LOAD(7); B_LOAD(8); }
        if constexpr (u >= 1 && u < 5) { H_LOAD(7 + 2 * u); H_LOAD(8 + 2 * u); }
    });
    ZBAR(0);
    D_PRE(7);
    segment(HOOK(u)
        D_UNIT(u, 7, 9);
        if constexpr (u < 4) { B_LOAD(9 + 2 * u); B_LOAD(10 + 2 * u); }
        if constexpr (u == 4) B_LOAD(17);
    });
    ZBAR(0);
    D_PRE(9);
    segment(HOOK(u)
        D_UNIT(u, 9, 17);
        if constexpr (u < 2) { B_LOAD(18 + 2 * u); B_LOAD(19 + 2 * u); }
        if constexpr (u == 2 || u == 3) { H_LOAD(13 + 2 * u); H_LOAD(14 + 2 * u); }
        if constexpr (u == 4) H_LOAD(21);
    });
    ZBAR(0);
    D_PRE(17);
    segment(HOOK(u)
        D_UNIT(u, 17, 22);
        if constexpr (u < 5) { B_LOAD(22 + 2 * u); B_LOAD(23 + 2 * u); }
        if constexpr (u >= 5 && u < 8) { H_LOAD(12 + 2 * u); H_LOAD(13 + 2 * u); }
    });
    ZBAR(63);
    u32x4 xv;
    const unsigned xo = (unsigned)((tb * 64 + w * 8 + (lane2 >> 3)) * 1024 + cb * 128 + (lane2 & 7) * 16);
    segment(HOOK(u)
        if constexpr (u < 2) { B_LOAD(32 + 2 * u); B_LOAD(33 + 2 * u); }
        if constexpr (u == 2) gload16(xv, (gcptr)x_, xo);
    });
#undef H_LOAD
#undef D_UNIT
#undef D_PRE
    ob = MFMA32(v1, PB(p1), ob);
#undef HOOK
#undef HALF
#undef EXPR
    {
        const f32x16 snd = G ? oa : ob;
        const lptr ex = smem + L_EXCH + w * 3072 + lin;
        *(LAS f32x4*)(ex) = f32x4{snd[0], snd[1], snd[2], snd[3]};
        *(LAS f32x4*)(ex + 1024) = f32x4{snd[4], snd[5], snd[6], snd[7]};
        *(LAS float*)(ex + 2048) = snd[8];
    }
    asm volatile("s_waitcnt vmcnt(0) lgkmcnt(0)\n\ts_barrier" ::: "memory");
    {
        f32x16 o = G ? ob : oa;
        const lptr ex = smem + L_EXCH + (w ^ 4) * 3072 + lin;
        const f32x4 e0 = *(LAS const f32x4*)(ex), e1 = *(LAS const f32x4*)(ex + 1024);
        const float e2 = *(LAS const float*)(ex + 2048);
#pragma unroll
        for (int r = 0; r < 4; ++r) { o[r] += e0[r]; o[4 + r] += e1[r]; }
        o[8] += e2;
        bool ok = o[8] <= 1.0995116e12f && o[8] >= 0.25f;
#pragma unroll
        for (int r = 0; r < 8; ++r) ok = ok && (__builtin_fabsf(o[r]) <= 3.0e38f);
        if (__builtin_expect(!__all(ok), 0)) attn_slow(rs_ws, head, G ? qb : qa, lane2, o);
        const float rl = __builtin_amdgcn_rcpf(o[8]);
        gstore16_dev(ws + (WS_O + (unsigned)((qblk * 8 + (w & 3) * 2 + G) * 32 + head) * 1024), lin, pack8(o, 0, rl));
    }
    PIN(xv);
    SFOR(m, 6) wf[m] = D_FRAG(m & 1, 22 + (m >> 1)); SEND
    SFOR(n, 12)
        PIN(hf[22 + (n >> 1)]);
        dd[n & 1] = MFMA32(wf[n % 6], PB(hf[22 + (n >> 1)]), dd[n & 1]); PIN(dd[n & 1]);
        if constexpr (n < 6) wf[n] = D_FRAG(n & 1, 25 + (n >> 1));
        SB();
    SEND
#undef D_FRAG
    WAITV(0); BAR();
    if (threadIdx.x == 0) __hip_atomic_store((gu64*)(ws + WS_F3 + (qblk * 32 + head) * 8), epoch, RLX_AGENT);
    const int prow = (w & 1) * 32 + (lane2 & 31);
    const lptr pw = smem + (kq < 2 ? kq * 16384 : 81920 + (kq - 2) * 32768) + prow * 256;
    const int psh = kq < 2 ? 32768 : 16384;
    const int rrow = w * 8 + (lane2 >> 3);
    const lptr pr0 = smem + rrow * 256 + (((lane2 & 7) * 2) ^ (rrow & 7)) * 16, pr1 = smem + rrow * 256 + (((lane2 & 7) * 2 + 1) ^ (rrow & 7)) * 16;
    const lptr pr2 = lbase(pr0 + 81920), pr3 = lbase(pr1 + 81920);
#define PART_WRITE(A, off) do { _Pragma("unroll") for (int t = 0; t < 2; ++t) _Pragma("unroll") for (int g = 0; g < 4; ++g) \
        *(LAS f32x4*)(pw + (off) + ((t * 8 + g * 2 + (lane2 >> 5)) ^ (prow & 7)) * 16) = f32x4{A[t][4 * g], A[t][4 * g + 1], A[t][4 * g + 2], A[t][4 * g + 3]}; } while (0)
#define PART_READ(P, Q, o1, o2) ((*(LAS const f32x4*)((P) + (o1)) + *(LAS const f32x4*)((P) + (o1) + 16384)) + (*(LAS const f32x4*)((Q) + (o2)) + *(LAS const f32x4*)((Q) + (o2) + 32768)))
    bf16x8 wo_f[16];
    {
        const int adj = (lane2 >> 5) * 512;
#pragma unroll
        for (int t = 0; t < 2; ++t) { db[t][0] = lbase(db[t][0] + adj); db[t][1] = lbase(db[t][1] + adj); db[t][2] = lbase(db[t][2] + adj); }
        SFOR(n, 16)
            constexpr int ps = B_POS(28 + (n >> 1));
            wo_f[n] = cat4(trd(db[n & 1][ps >> 3] + (ps & 7) * 8192), trd(db[n & 1][ps >> 3] + (ps & 7) * 8192 + 512));
        SEND
    }
    PART_WRITE(dd, 0);
    const unsigned f3o = (unsigned)(((tb >> 2) * 32 + (lane2 & 31)) * 8);
    if (w == 1 && blockIdx.x == 0) {
        u32x4 e0, e1;
        gload16_dev(e0, (gcptr)ws + WS_F2, lin); gload16_dev(e1, (gcptr)ws + WS_F2 + 1024, lin);
        WAITV(0); PIN(e0); PIN(e1);
        const unsigned el = (unsigned)epoch, eh = (unsigned)(epoch >> 32);
        if (!__all(e0[0] == el && e0[2] == el && e1[0] == el && e1[2] == el && e0[1] == eh && e0[3] == eh && e1[1] == eh && e1[3] == eh)) {
#pragma nounroll
            for (int q = 0; q < 4; ++q) poll_flags((gcptr)ws + WS_F2, (unsigned)(q * 64 + lane2) * 8, epoch);
        }
    }
    if (w == 0) poll_flags((gcptr)ws + WS_F3, f3o, epoch);
    asm volatile("s_waitcnt vmcnt(0) lgkmcnt(0)\n\ts_barrier" ::: "memory");
    u32x4 of[8];
    {
        gcptr op = (gcptr)ws + WS_O + (size_t)(tt4 * 32 + kq) * 1024;
#pragma unroll
        for (int m = 0; m < 8; ++m) gload16_dev(of[m], op + ((m + cb) & 7) * 4096, lin);
    }
    f32x4 glo = PART_READ(pr0, pr2, 0, 0), ghi = PART_READ(pr1, pr3, 0, 0);
#pragma unroll
    for (int e = 0; e < 4; ++e) {
        glo[e] = glo[e] * __builtin_amdgcn_rcpf(1.f + __builtin_amdgcn_exp2f(glo[e] * -1.4426950408889634f));
        ghi[e] = ghi[e] * __builtin_amdgcn_rcpf(1.f + __builtin_amdgcn_exp2f(ghi[e] * -1.4426950408889634f));
    }
    PIN(glo); PIN(ghi);
    f32x16 ss[2] = {};
#define O_STEP(m, N) do { WAITV(N); PIN(of[m]); ss[0] = MFMA32(wo_f[2 * (m)], PB(of[m]), ss[0]); ss[1] = MFMA32(wo_f[2 * (m) + 1], PB(of[m]), ss[1]); PIN(ss[0]); PIN(ss[1]); } while (0)
    O_STEP(0, 7); O_STEP(1, 6); O_STEP(2, 5); O_STEP(3, 4); O_STEP(4, 3); O_STEP(5, 2); O_STEP(6, 1); O_STEP(7, 0);
#undef O_STEP
    PART_WRITE(ss, psh);
    asm volatile("s_waitcnt lgkmcnt(0)\n\ts_barrier" ::: "memory");
    const f32x4 slo = PART_READ(pr0, pr2, 32768, 16384), shi = PART_READ(pr1, pr3, 32768, 16384);
    const u32x4 ovv = {pk2(__builtin_fmaf(slo[0], glo[0], bf_lo(xv[0])), __builtin_fmaf(slo[1], glo[1], bf_hi(xv[0]))),
                      pk2(__builtin_fmaf(slo[2], glo[2], bf_lo(xv[1])), __builtin_fmaf(slo[3], glo[3], bf_hi(xv[1]))),
                      pk2(__builtin_fmaf(shi[0], ghi[0], bf_lo(xv[2])), __builtin_fmaf(shi[1], ghi[1], bf_hi(xv[2]))),
                      pk2(__builtin_fmaf(shi[2], ghi[2], bf_lo(xv[3])), __builtin_fmaf(shi[3], ghi[3], bf_hi(xv[3])))};
    gstore16_dev((gptr)out_, xo, ovv);
    if (threadIdx.x == 64 && blockIdx.x == 0) __hip_atomic_store((gu64*)(ws + WS_N), epoch, RLX_AGENT);
    asm volatile(".Lcode_end:");
}

extern "C" void kernel_launch(void* const* d_in, const int* in_sizes, int n_in,
                              void* d_out, int out_size, void* d_ws, size_t ws_size,
                              hipStream_t stream) {
    fused_block<<<256, 512, 0, stream>>>((const char*)d_in[0], (const char*)d_in[1], (const char*)d_in[2], (const char*)d_in[4],
                                         (const char*)d_in[5], (const char*)d_in[6], (char*)d_ws, (char*)d_out);
}
```
